```python
import torch

def _softmax(x, axis=-1):
    return torch.softmax(x, dim=axis)
def _gelu(x):
    return torch.nn.functional.gelu(x, approximate="tanh")
def _silu(x):
    return torch.nn.functional.silu(x)
def _sigmoid(x):
    return torch.sigmoid(x)
def _layernorm(x, axis=-1, eps=1e-5):
    m = x.mean(dim=axis, keepdim=True)
    v = x.var(dim=axis, keepdim=True, unbiased=False)
    return (x - m) / torch.sqrt(v + eps)
def _scatter_add(tgt, idx, src, axis=0):
    return tgt.clone().index_add_(axis, idx, src)
def _segment_sum(src, seg, n):
    out = torch.zeros((n,) + src.shape[1:], dtype=src.dtype)
    return out.index_add_(0, seg, src)
def _pad(x, pw):
    flat = tuple(v for pair in reversed(pw) for v in pair)
    return torch.nn.functional.pad(x, flat)

kernel_name = 'standalone__mha_block_cached__near_ridge'

def setup_inputs():

    _w = (lambda a: (lambda t: t.to(torch.bfloat16) if t.is_floating_point() else t)(torch.as_tensor(a)))
    B, Sq, D = 8, 96, 1024
    H, Sk, Dh = 32, 512, 128
    return {
        "x": _w((lambda *s: torch.randn(*s, dtype=torch.bfloat16) * 0.8)(B, Sq, D)),
        "k_cache": _w((lambda *s: torch.randn(*s, dtype=torch.bfloat16) * 0.8)(B, H, Sk, Dh)),
        "v_cache": _w((lambda *s: torch.randn(*s, dtype=torch.bfloat16) * 0.05)(B, H, Sk, Dh)),
        "wq": _w((lambda *s: torch.randn(*s, dtype=torch.bfloat16) * 0.8)(D, H * Dh) * (D ** -0.5)),
        "wo": _w((lambda *s: torch.randn(*s, dtype=torch.bfloat16) * 0.05)(H * Dh, D) * ((H * Dh) ** -0.5)),
    }

def reference(x, k_cache, v_cache, wq, wo):
    B, Sq, D = x.shape
    H, Dh = 32, 128
    q = (lambda x, *d: x.permute(*d))((x @ wq).reshape(B, Sq, H, Dh), 0, 2, 1, 3)
    scores = torch.einsum("bhqd,bhkd->bhqk", q, k_cache) * (Dh ** -0.5)
    p = _softmax(scores, axis=-1)
    o = torch.einsum("bhqk,bhkd->bhqd", p, v_cache)
    return (lambda x, *d: x.permute(*d))(o, 0, 2, 1, 3).reshape(B, Sq, H * Dh) @ wo
```

```cpp
#define PRETOUCH 0
#define READOUT 0
#include <hip/hip_runtime.h>
#include <hip/hip_bf16.h>

typedef unsigned v4u __attribute__((ext_vector_type(4)));
typedef unsigned v2u __attribute__((ext_vector_type(2)));
typedef short bf16x8 __attribute__((ext_vector_type(8)));
typedef short s16x4 __attribute__((ext_vector_type(4)));
typedef float f32x16 __attribute__((ext_vector_type(16)));
typedef float f32x4 __attribute__((ext_vector_type(4)));
typedef float f32x2 __attribute__((ext_vector_type(2)));
typedef __bf16 bf16x2 __attribute__((ext_vector_type(2)));
typedef unsigned long long u64;
typedef __amdgpu_buffer_rsrc_t rsrc_t;
#define LAS __attribute__((address_space(3)))
#define GAS __attribute__((address_space(1)))
#define DI __device__ __forceinline__
typedef LAS char* lptr;

#define POL_KV " nt"
#define THR 8.f
#define POS_B 4
#define PBF 2
#define WOTHR 8
#define PSLEEP 1
#define CODE_BYTES 28672
#define HSLEEP 4
#define ADELAY 40
#define FSWAP 0
#define PARK 1
#define STAMP 0

constexpr int WSLOT = 16384, XSLOT = 12288;
constexpr int LDS_W = 0, LDS_X = 7 * WSLOT, LDS_BYTES = LDS_X + 4 * XSLOT;
constexpr int LDS_M = LDS_BYTES - 256;
constexpr u64 SLOT_TAB = 0x2106543872106543ull;
constexpr int M_FULL = 0, M_EPOCH = 2, M_PROG = 8;
constexpr size_t WS_EPOCH = 0;
constexpr size_t WS_FLAG1 = 4096;
constexpr size_t WS_FLAG2 = WS_FLAG1 + 32 * 256;
constexpr size_t WS_O = 131072;
constexpr size_t WS_PART = WS_O + 256 * 24576;
constexpr size_t WS_STAMP = WS_PART + 256 * 4 * 12288;
constexpr unsigned SPIN_CAP = 1u << 18;

DI unsigned pk2(float a, float b) { f32x2 v = {a, b}; return __builtin_bit_cast(unsigned, __builtin_convertvector(v, bf16x2)); }
DI unsigned pk2s(float a, float b, float s) { f32x2 v = {a, b}; v *= s; return __builtin_bit_cast(unsigned, __builtin_convertvector(v, bf16x2)); }
DI float max2(float a, float b) { return __builtin_elementwise_maximum(a, b); }
DI float max3(float a, float b, float c) { return max2(max2(a, b), c); }
DI unsigned umin(unsigned a, unsigned b) { return a < b ? a : b; }
DI unsigned lds_ld(lptr sm, int word) {
    unsigned r; const unsigned a = (unsigned)(size_t)(sm + LDS_M) + 4u * (unsigned)word;
    asm volatile("ds_read_b32 %0, %1\n\ts_waitcnt lgkmcnt(0)" : "=v"(r) : "v"(a) : "memory"); return r; }
DI v2u lds_ld2(lptr sm, int word) {
    v2u r; const unsigned a = (unsigned)(size_t)(sm + LDS_M) + 4u * (unsigned)word;
    asm volatile("ds_read_b64 %0, %1\n\ts_waitcnt lgkmcnt(0)" : "=v"(r) : "v"(a) : "memory"); return r; }
DI unsigned lds_full(lptr sm) { const v2u r = lds_ld2(sm, M_FULL); return __builtin_amdgcn_readfirstlane(umin(r[0], r[1])); }
DI void lds_st(lptr sm, int word, unsigned v) {
    const unsigned a = (unsigned)(size_t)(sm + LDS_M) + 4u * (unsigned)word;
    asm volatile("ds_write_b32 %0, %1" :: "v"(a), "v"(v) : "memory"); }
DI void lds_max(lptr sm, int word, unsigned v, int lane) {
    const unsigned a = (unsigned)(size_t)(sm + LDS_M) + 4u * (unsigned)word;
    if (lane == 0) asm volatile("ds_max_u32 %0, %1" :: "v"(a), "v"(v) : "memory"); }
#define CFENCE() asm volatile("" ::: "memory")
#define SCHED0() __builtin_amdgcn_sched_barrier(0)
#define WAIT_LGKM(n) do { __builtin_amdgcn_s_waitcnt(0xC07F | ((n) << 8)); asm volatile("" ::: "memory"); } while (0)
#define WAIT_VM(n) asm volatile("s_waitcnt vmcnt(%0)" :: "i"(n) : "memory")
#define BAR() asm volatile("s_barrier" ::: "memory")
#define PIN(x) asm volatile("" : "+v"(x))
#define MFMA32(a, b, c) __builtin_amdgcn_mfma_f32_32x32x16_bf16(a, b, c, 0, 0, 0)
#define RLX_AGENT __ATOMIC_RELAXED, __HIP_MEMORY_SCOPE_AGENT
DI void put_flag(GAS u64* p, u64 v) { if (FSWAP) (void)__hip_atomic_exchange(p, v, RLX_AGENT); else __hip_atomic_store(p, v, RLX_AGENT); }

DI void wait_full(lptr sm, unsigned need, unsigned& cur) {
    unsigned spins = 0;
#pragma nounroll
    while (__builtin_expect(cur < need, 0)) {
        cur = lds_full(sm);
        if (cur >= need || ++spins > SPIN_CAP) break;
        __builtin_amdgcn_s_sleep(1);
    }
    CFENCE();
}
DI void wait_prog(lptr sm, unsigned need, int lane) {
    unsigned spins = 0;
#pragma nounroll
    while (__builtin_amdgcn_ballot_w64(lds_ld(sm, M_PROG + (lane & 3)) < need) != 0ull && ++spins <= SPIN_CAP) __builtin_amdgcn_s_sleep(6);
    CFENCE();
}
DI void wait_flags(GAS u64* fl, u64 epoch) {
    unsigned spins = 0;
#pragma nounroll
    for (;;) {
        const u64 f = __hip_atomic_load(fl, RLX_AGENT);
        if (__builtin_amdgcn_ballot_w64(f != epoch) == 0ull || ++spins > SPIN_CAP) break;
        __builtin_amdgcn_s_sleep(PSLEEP);
    }
    CFENCE();
}
#define TSTAMP(k) do { if ((k) == 0) rt0 = __builtin_amdgcn_s_memrealtime(); if ((k) == READOUT) rt1 = __builtin_amdgcn_s_memrealtime(); } while (0)
#define TDUMP() do {} while (0)

DI v4u mk_srd(const void* p, unsigned bytes) { const u64 a = (u64)p; return v4u{(unsigned)a, (unsigned)(a >> 32) & 0xffffu, bytes, 0x00020000u}; }
#define DMA4(POL, srd, dst, v, soff) asm volatile("s_mov_b32 m0, %0\n\ts_nop 0\n\tbuffer_load_dwordx4 %3, %1, %2 offen" POL " lds\n\tbuffer_load_dwordx4 %4, %1, %2 offen offset:1024" POL " lds\n\t" \
        "buffer_load_dwordx4 %5, %1, %2 offen offset:2048" POL " lds\n\tbuffer_load_dwordx4 %6, %1, %2 offen offset:3072" POL " lds" \
        :: "s"((unsigned)(dst)), "s"(srd), "s"((unsigned)(soff)), "v"(v[0]), "v"(v[1]), "v"(v[2]), "v"(v[3]) : "memory")
#define DMA3(POL, srd, dst, v, soff) asm volatile("s_mov_b32 m0, %0\n\ts_nop 0\n\tbuffer_load_dwordx4 %3, %1, %2 offen" POL " lds\n\tbuffer_load_dwordx4 %4, %1, %2 offen offset:1024" POL " lds\n\t" \
        "buffer_load_dwordx4 %5, %1, %2 offen offset:2048" POL " lds" \
        :: "s"((unsigned)(dst)), "s"(srd), "s"((unsigned)(soff)), "v"(v[0]), "v"(v[1]), "v"(v[2]) : "memory")

struct GFrag { bf16x8 xa[12]; s16x4 wl[4], wh[4]; };
#define GM(F, m, Z) (acc[(m) >> 1] = __builtin_amdgcn_mfma_f32_16x16x32_bf16(__builtin_shufflevector(F.wl[(m) & 3], F.wh[(m) & 3], 0, 1, 2, 3, 4, 5, 6, 7), F.xa[2 * ((m) >> 2) + ((m) & 1)], \
                                                                             (Z) && !((m) & 1) ? f32x4{} : acc[(m) >> 1], 0, 0, 0))
#define GMA(F, m, Z) do { const bf16x8 a_ = __builtin_shufflevector(F.wl[(m) & 3], F.wh[(m) & 3], 0, 1, 2, 3, 4, 5, 6, 7); \
        if ((Z) && !((m) & 1)) asm volatile("v_mfma_f32_16x16x32_bf16 %0, %1, %2, 0" : "=&v"(acc[(m) >> 1]) : "v"(a_), "v"(F.xa[2 * ((m) >> 2) + ((m) & 1)])); \
        else asm volatile("v_mfma_f32_16x16x32_bf16 %0, %1, %2, %0" : "+v"(acc[(m) >> 1]) : "v"(a_), "v"(F.xa[2 * ((m) >> 2) + ((m) & 1)])); } while (0)
#define GADDR() do { wa0 = wsl * WSLOT + woff; wa1 = wa0 ^ wx; xa0 = xsl * XSLOT + xlo; xa1 = xa0 ^ 64u; xa2 = xsl * XSLOT + xhi; xa3 = xa2 ^ 64u; PIN(wa0); PIN(wa1); PIN(xa0); PIN(xa1); PIN(xa2); PIN(xa3); } while (0)
#define GSTEP(RING, DOBAR, FCUR, FPREV, HASPREV, Z) do { \
        if (DOBAR) BAR(); \
        SCHED0(); \
        _Pragma("unroll") for (int m = 0; m < 24; ++m) { \
            if (HASPREV) GMA(FPREV, m, Z); \
            SCHED0(); \
            if (m < 8) { const lptr wt = (lptr)(size_t)((m & 4) ? wa1 : wa0) + ((m >> 1) & 1) * 32 * 256 + (m & 1) * 4 * 256; \
                if (m & 1) FCUR.wh[m >> 1] = __builtin_amdgcn_ds_read_tr16_b64_v4i16((LAS s16x4*)wt); else FCUR.wl[m >> 1] = __builtin_amdgcn_ds_read_tr16_b64_v4i16((LAS s16x4*)wt); } \
            else if (m < 20) FCUR.xa[m - 8] = *(const LAS bf16x8*)((lptr)(size_t)(m < 14 ? ((m & 1) ? xa1 : xa0) : ((m & 1) ? xa3 : xa2)) + (((m - 8) >> 1) % 3) * 2048); \
            else if (m == 20) { wsl = (wsl + 1 == (RING)) ? 0 : wsl + 1; xsl = (xsl + 1) & 3; wa0 = wsl * WSLOT + woff; wa1 = wa0 ^ wx; PIN(wa0); PIN(wa1); } \
            else if (m == 21) { xa0 = xsl * XSLOT + xlo; xa1 = xa0 ^ 64u; PIN(xa0); PIN(xa1); } \
            else if (m == 22) { xa2 = xsl * XSLOT + xhi; xa3 = xa2 ^ 64u; PIN(xa2); PIN(xa3); } \
            SCHED0(); } \
        WAIT_LGKM(12); } while (0)

__global__ __launch_bounds__(512, 2) void mha_fused(const char* __restrict__ xg, const char* __restrict__ kg, const char* __restrict__ vg,
                                                    const char* __restrict__ wqg, const char* __restrict__ wog, char* __restrict__ outg, char* __restrict__ ws) {
    __shared__ __attribute__((aligned(16))) char smem[LDS_BYTES];
    const lptr sm = (lptr)smem;
    const unsigned smb = (unsigned)(size_t)sm;
    const int tid = threadIdx.x, lane = tid & 63, w = __builtin_amdgcn_readfirstlane(tid >> 6);
    const int wg = blockIdx.x, xcd = wg & 7, idx = wg >> 3;
    const int hg = xcd & 3, b = 4 * (xcd >> 2) + (idx >> 3), hl = idx & 7, h = 8 * hg + hl;
    const int nt = (hl - (b & 1) - 2 * (b & 2)) & 7;
    const int bh = b * 32 + h;
    const int cls = hl & 1;
    const int rw = lane >> 4, ch = lane & 15, lw = w & 3;
    const int r = lane & 31, hh = lane >> 5, blk = (lane >> 4) & 1, q4 = (lane & 15) >> 2, p = lane & 3;
    u64 epoch = 0, rt0 = 0, rt1 = 0;
    u64 code0; asm volatile("s_getpc_b64 %0" : "=s"(code0)); code0 &= ~(u64)255;
    if (w == 0) TSTAMP(0);

#define PT() do { if (w == 3) {     \
        const char* p_ = lane < 32 ? wog + (size_t)hg * 1024 * 2048 + lane * 65536 + nt * 256 \
            : lane < 36 ? ws + WS_O + (size_t)(b * 32 + hg * 8) * 24576 + (lane - 32) * 65280 \
            : lane < 44 ? ws + WS_PART + (size_t)(b * 32 + hl + 8 * ((lane - 36) >> 1)) * 4 * 12288 + (lane & 1) * 49024 \
            : lane < 48 ? outg + (size_t)b * 96 * 2048 + (lane - 44) * 65280 \
            : lane < 56 ? kg + (size_t)bh * 131072 + (lane & 1) * 65536 : vg + (size_t)bh * 131072 + (lane & 1) * 65536; \
        const unsigned v_ = __hip_atomic_load((GAS unsigned*)p_, RLX_AGENT); asm volatile("" :: "v"(v_)); } } while (0)
    if (__builtin_expect(w >= 4, 1)) {
        const int rot = (2 * hl + 4 * (b & 3)) & 15;
        const v4u rq = mk_srd(wqg + h * 256 + lw * 16 * 8192, 1024 * 8192 - (h * 256 + lw * 16 * 8192));
        const v4u rx = mk_srd(xg + (size_t)(b * 96 + lw * 24) * 2048, 24 * 2048);
        unsigned vq[4], vx[3];
#pragma unroll
        for (int i = 0; i < 4; ++i) vq[i] = (4 * i + rw) * 8192 - i * 1024 + ((ch ^ (2 * rw + 8 * (i >> 1))) << 4);
#pragma unroll
        for (int i = 0; i < 3; ++i) vx[i] = (8 * i + (lane >> 3)) * 2048 - i * 1024 + (((lane & 7) ^ (4 * ((3 * lw + i) & 1) + rw)) << 4);
        const unsigned wdst = smb + LDS_W + lw * 4096, xdst = smb + LDS_X + lw * 3072;
#define P1_ISSUE(i) do { const unsigned kt_ = (unsigned)((i) + rot) & 15u; \
            DMA3("", rx, xdst + ((i) & 3) * XSLOT, vx, kt_ * 128u); \
            DMA4("", rq, wdst + (((i) + 2) % 3) * WSLOT, vq, kt_ * (64u * 8192u)); } while (0)
        P1_ISSUE(0);
        SCHED0();
        __builtin_amdgcn_s_setprio(2);
        if (w == 7) TSTAMP(24);
        P1_ISSUE(1);
        const bool early = w >= 6 && cls == 1;
        const v4u rk = mk_srd(kg + (size_t)bh * 131072 - 1024, 131072 + 1024), rv = mk_srd(vg + (size_t)bh * 131072 - 1024, 131072 + 1024);
        unsigned vk[4];
#pragma unroll
        for (int i = 0; i < 4; ++i) vk[i] = (8 * (i >> 1) + ((lane >> 2) & 7)) * 256 + (i & 1) * 128 + (lane >> 5) * 64 + (((lane & 3) ^ ((2 * (i >> 1) + ((lane >> 4) & 1)) & 3)) << 4) - i * 1024 + 1024;
        const unsigned hw = (w & 1) * 8192;
#define K_ISSUE(slot, j) do { DMA4(POL_KV, rk, smb + LDS_W + (slot) * WSLOT + hw, vk, (j) * 16384 + hw); DMA4(POL_KV, rk, smb + LDS_W + (slot) * WSLOT + hw + 4096, vk, (j) * 16384 + hw + 4096); } while (0)
#define V_ISSUE(slot, j) do { DMA4(POL_KV, rv, smb + LDS_W + (slot) * WSLOT + hw, vk, (j) * 16384 + hw); DMA4(POL_KV, rv, smb + LDS_W + (slot) * WSLOT + hw + 4096, vk, (j) * 16384 + hw + 4096); } while (0)
        v4u park[3][8];
#define PARK_LD4(d, o, srd, v, soff) asm volatile("buffer_load_dwordx4 %0, %6, %4, %5 offen" POL_KV "\n\tbuffer_load_dwordx4 %1, %7, %4, %5 offen offset:1024" POL_KV "\n\t" \
            "buffer_load_dwordx4 %2, %8, %4, %5 offen offset:2048" POL_KV "\n\tbuffer_load_dwordx4 %3, %9, %4, %5 offen offset:3072" POL_KV \
            : "=&v"(d[o]), "=&v"(d[o + 1]), "=&v"(d[o + 2]), "=&v"(d[o + 3]) : "s"(srd), "s"((unsigned)(soff)), "v"(v[0]), "v"(v[1]), "v"(v[2]), "v"(v[3]) : "memory")
#define PARK_LD(n, srd, v, j) do { PARK_LD4(park[n], 0, srd, v, (j) * 16384 + hw); PARK_LD4(park[n], 4, srd, v, (j) * 16384 + hw + 4096); } while (0)
#define PARK_WR(n) do { _Pragma("unroll") for (int i_ = 0; i_ < 8; ++i_) *(LAS v4u*)(sm + LDS_W + (n) * WSLOT + hw + i_ * 1024 + lane * 16) = park[n][i_]; } while (0)
        WAIT_VM(7); BAR();
        P1_ISSUE(2);
#pragma unroll
        for (int i = 1; i < 14; ++i) {
            if ((i == POS_B + 1 || i == POS_B + 2) && early) WAIT_VM(PARK ? 63 : 39); else WAIT_VM(7);
            BAR();
            P1_ISSUE(i + 2);
            if (i == POS_B && early) { K_ISSUE(3, 0); K_ISSUE(4, 1); V_ISSUE(5, 0); K_ISSUE(6, 2);
                if (PARK) { WAIT_VM(32); PARK_LD(0, rv, vk, 1); PARK_LD(1, rk, vk, 3); PARK_LD(2, rv, vk, 2); } }
        }
        const unsigned ord = cls ? 0x64275310u : 0x75316420u;
#define KT(i) ((((unsigned)hl + (ord >> (4u * ((unsigned)(i) >> 1)))) & 7u) * 2u + ((unsigned)(i) & 1u))
        const v4u ro = mk_srd(wog + (size_t)hg * 1024 * 2048 + nt * 256, 1024 * 2048 - nt * 256);
        unsigned vo[4];
#pragma unroll
        for (int i = 0; i < 4; ++i) vo[i] = (4 * i + rw) * 2048 - i * 1024 + ((ch ^ (2 * rw + 8 * (i >> 1))) << 4);
        const v4u rO = mk_srd(ws + WS_O + (size_t)(b * 32 + hg * 8) * 24576, 8 * 24576);
        GAS u64* f1 = (GAS u64*)(ws + WS_FLAG1 + (size_t)(b * 4 + hg) * 256);
        GAS u64* f1a = f1 + (lane & 31);
        unsigned rdy = 0;
#define HEAD_OK(n) (((rdy >> (2u * KT(2 * (n)))) & 15u) == 15u)
#define HEAD_WAIT(n) do { unsigned sp_ = 0; _Pragma("nounroll") while (!HEAD_OK(n) && ++sp_ <= SPIN_CAP) { if (HSLEEP && sp_ > 1) __builtin_amdgcn_s_sleep(HSLEEP); rdy = (unsigned)__builtin_amdgcn_ballot_w64(__hip_atomic_load(f1a, RLX_AGENT) == epoch); } CFENCE(); } while (0)
        if (w < 6) {
            WAIT_VM(7); BAR();
            WAIT_VM(0); BAR();
            BAR();
            if (w == 4 && lane < 16 && lane != M_EPOCH && lane != M_EPOCH + 1) lds_st(sm, lane, lane == M_PROG + 3 ? 1000u : 0u);
            WAIT_LGKM(0);
            BAR();
            if (w == 4) {
                const v2u e = lds_ld2(sm, M_EPOCH); epoch = ((u64)__builtin_amdgcn_readfirstlane(e[1]) << 32) | (unsigned)__builtin_amdgcn_readfirstlane(e[0]);
                unsigned v0[4];
#pragma unroll
                for (int i = 0; i < 4; ++i) v0[i] = lane * 16;
#define O_WHOLE(i) do { _Pragma("unroll") for (int g_ = 0; g_ < 3; ++g_) DMA4(" sc1", rO, smb + LDS_X + (i) * XSLOT + g_ * 4096, v0, KT(i) * (unsigned)XSLOT + g_ * 4096); } while (0)
                bool ok = false; u64 fv1 = 0; PIN(fv1);
                if (cls) {
                    unsigned spins = 0, pv;
#pragma nounroll
                    while (__builtin_amdgcn_ballot_w64(lds_ld(sm, M_PROG + (lane & 3)) < 16u) != 0ull && ++spins <= SPIN_CAP) __builtin_amdgcn_s_sleep(8);
                    asm volatile("global_load_dwordx2 %0, %1, off sc1" : "+v"(fv1) : "v"(f1a) : "memory");
#pragma nounroll
                    for (;;) {
                        unsigned st; asm volatile("s_getreg_b32 %0, hwreg(HW_REG_IB_STS, 0, 4)" : "=s"(st) :: "memory");
                        if (st == 0u) {
                            asm volatile("s_waitcnt vmcnt(0)" : "+v"(fv1) :: "memory");
                            rdy = (unsigned)__builtin_amdgcn_ballot_w64(fv1 == epoch); ok = HEAD_OK(1);
                            if (ok) break;
                            __builtin_amdgcn_s_sleep(4);
                            asm volatile("global_load_dwordx2 %0, %1, off sc1" : "+v"(fv1) : "v"(f1a) : "memory");
                        }
                        __builtin_amdgcn_s_sleep(1);
                        pv = lds_ld(sm, M_PROG + (lane & 3));
                        if (__builtin_amdgcn_ballot_w64(pv == 17u) != 0ull || ++spins > SPIN_CAP) break;
                    }
                    if (ok) O_WHOLE(2);
                    *(volatile LAS unsigned*)sm = (ok && HEAD_OK(2)) ? 1u : 0u;
                    WAIT_LGKM(0);
                }
                BAR();
                if (!ok) {
                    if (ADELAY && !cls) __builtin_amdgcn_s_sleep(ADELAY);
                    asm volatile("s_waitcnt vmcnt(0)" : "+v"(fv1) :: "memory");
                    rdy = (unsigned)__builtin_amdgcn_ballot_w64(fv1 == epoch); HEAD_WAIT(1);
                    O_WHOLE(2);
                }
                TSTAMP(11);
                O_WHOLE(3);
                HEAD_WAIT(2);
                WAIT_VM(12);
            } else BAR();
        } else {
            if (!early) { K_ISSUE(3, 0); WAIT_VM(15); } else WAIT_VM(7);
            BAR();
            if (!early) { K_ISSUE(4, 1); WAIT_VM(16); } else if (PARK) WAIT_VM(0); else { V_ISSUE(0, 1); WAIT_VM(8); }
            BAR();
            if (!early) V_ISSUE(5, 0); else if (!PARK) K_ISSUE(1, 3);
            BAR();
            if (!early) K_ISSUE(6, 2); else if (PARK) { PARK_WR(0); PARK_WR(1); PARK_WR(2); }
            WAIT_LGKM(0);
            BAR();
            const unsigned pa_ = smb + LDS_M + 4u * (M_PROG + (lane & 3)), fa_ = smb + LDS_M + 4u * (M_FULL + (w & 1));
            {
                const unsigned t0 = early ? (PARK ? 7u : 6u) : 4u;
                const u64 kb_ = (u64)(kg + (size_t)bh * 131072 + 16384 - 1024 + hw), vb_ = (u64)(vg + (size_t)bh * 131072 - 1024 + hw);
                unsigned pv, tv;
                asm volatile(
                    "s_mov_b32 s88, %[t0]\n\ts_mov_b64 s[90:91], %[tab]\n\ts_mov_b32 s94, %[nrec]\n\ts_mov_b32 s95, 0x20000\n\ts_mov_b32 s97, %[cap]\n"
                    "1:\n\ts_waitcnt vmcnt(15)\n\ts_sub_i32 s89, s88, 7\n\ts_cmp_eq_u32 s88, 7\n\ts_addc_u32 s89, s89, 0\n"
                    "2:\n\ts_getreg_b32 s96, hwreg(HW_REG_IB_STS, 0, 4)\n\tds_read_b32 %[pv], %[pa]\n\ts_add_u32 s96, s96, 7\n\ts_lshr_b32 s96, s96, 3\n\ts_sub_u32 s96, s88, s96\n\t"
                    "v_mov_b32 %[tv], s96\n\tds_write_b32 %[fa], %[tv]\n\ts_waitcnt lgkmcnt(1)\n\tv_cmp_gt_i32 vcc, s89, %[pv]\n\ts_cbranch_vccz 3f\n\t"
                    "s_sleep 2\n\ts_sub_u32 s97, s97, 1\n\ts_cbranch_scc0 2b\n\ts_mov_b32 s97, 0\n"
                    "3:\n\ts_and_b32 s96, s90, 15\n\ts_lshl_b32 s96, s96, 14\n\ts_add_u32 m0, s96, %[lb]\n\ts_bitcmp1_b32 %[km], s88\n\ts_cselect_b64 s[92:93], %[kb], %[vb]\n\t"
                    "s_sub_u32 s89, s88, 1\n\ts_lshl_b32 s89, s89, 13\n\ts_bitset0_b32 s89, 13\n\t"
                    "buffer_load_dwordx4 %[v0], s[92:95], s89 offen" POL_KV " lds\n\tbuffer_load_dwordx4 %[v1], s[92:95], s89 offen offset:1024" POL_KV " lds\n\t"
                    "buffer_load_dwordx4 %[v2], s[92:95], s89 offen offset:2048" POL_KV " lds\n\tbuffer_load_dwordx4 %[v3], s[92:95], s89 offen offset:3072" POL_KV " lds\n\t"
                    "s_add_u32 m0, m0, 0x1000\n\ts_bitset1_b32 s89, 12\n\t"
                    "buffer_load_dwordx4 %[v0], s[92:95], s89 offen" POL_KV " lds\n\tbuffer_load_dwordx4 %[v1], s[92:95], s89 offen offset:1024" POL_KV " lds\n\t"
                    "buffer_load_dwordx4 %[v2], s[92:95], s89 offen offset:2048" POL_KV " lds\n\tbuffer_load_dwordx4 %[v3], s[92:95], s89 offen offset:3072" POL_KV " lds\n\t"
                    "s_lshr_b64 s[90:91], s[90:91], 4\n\ts_add_u32 s88, s88, 1\n\ts_cmp_lt_u32 s88, 16\n\ts_cbranch_scc1 1b"
                    : [pv] "=&v"(pv), [tv] "=&v"(tv)
                    : [t0] "s"(t0), [tab] "s"(SLOT_TAB >> (4u * t0)), [nrec] "s"(131072u + 1024u - hw), [cap] "s"(SPIN_CAP), [pa] "v"(pa_), [fa] "v"(fa_), [lb] "s"(smb + hw), [km] "s"(0x2AAAu),
                      [kb] "s"(kb_), [vb] "s"(vb_), [v0] "v"(vk[0]), [v1] "v"(vk[1]), [v2] "v"(vk[2]), [v3] "v"(vk[3])
                    : "s88", "s89", "s90", "s91", "s92", "s93", "s94", "s95", "s96", "s97", "vcc", "scc", "memory");
            }
            if (w == 7) TSTAMP(25);
            const int fwd = M_FULL + (w & 1);
            WAIT_VM(16); lds_max(sm, fwd, 14u, lane);
            WAIT_VM(8); lds_st(sm, fwd, 15u);
            WAIT_VM(0); lds_st(sm, fwd, 16u);
            if (w == 7) TSTAMP(22);
#define WO_ISSUE2(i) do { const unsigned d_ = smb + LDS_W + (((i) + 3) % 7) * WSLOT + hw, so_ = (KT(i) * 64u + (w & 1) * 32u) * 2048u; \
            DMA4("", ro, d_, vo, so_); DMA4("", ro, d_ + 4096u, vo, so_ + 16u * 2048u); } while (0)
            wait_prog(sm, 12u, lane); WO_ISSUE2(0); WO_ISSUE2(1);
            wait_prog(sm, 14u, lane); WAIT_VM(8); WO_ISSUE2(2); WAIT_VM(8); WO_ISSUE2(3);
            WAIT_VM(16);
            if (w == 7) TSTAMP(23);
            BAR();
            WAIT_VM(8);
        }
        unsigned vl[3];
#pragma unroll
        for (int i = 0; i < 3; ++i) vl[i] = lane * 16 + lw * 3072;
#define O_ISSUE(i) DMA3(" sc1", rO, xdst + ((i) & 3) * XSLOT, vl, KT(i) * (unsigned)XSLOT)
#define WO_ISSUE(i) DMA4("", ro, wdst + (((i) + 3) % 7) * WSLOT, vo, (KT(i) * 64u + lw * 16u) * 2048u)
        u64 fv = 0; PIN(fv);
#define REFRESH(n) do { if (w == 4 && !HEAD_OK(n)) asm volatile("global_load_dwordx2 %0, %1, off sc1" : "+v"(fv) : "v"(f1a) : "memory"); } while (0)
        BAR();
        O_ISSUE(4); WO_ISSUE(4); REFRESH(3); WO_ISSUE(5); WAIT_VM(11); BAR();
#pragma unroll
        for (int i = 3; i < 13; ++i) {
            if (!(i & 1) && i < 12) REFRESH((i + 4) >> 1);
            O_ISSUE(i + 2); WO_ISSUE(i + 3); WAIT_VM(11);
            if ((i & 1) && w == 4) { PIN(fv); rdy |= (unsigned)__builtin_amdgcn_ballot_w64(fv == epoch); if (i == 5) TSTAMP(20); HEAD_WAIT((i + 3) >> 1); if (i == 5) TSTAMP(21); }
            BAR();
        }
        O_ISSUE(15); WAIT_VM(4 + 3); BAR();
        WAIT_VM(0); BAR();
        TDUMP();
        return;
    }
    {
        u64 eraw; unsigned cpf = 0;
        asm volatile("global_load_dwordx2 %0, %1, off sc1" : "=v"(eraw) : "v"(ws + WS_EPOCH + b * 256) : "memory");
        const unsigned woff = smb + LDS_W + (8 * rw + q4) * 256 + (((4 * w + (p >> 1)) ^ (2 * q4 + 8 * (rw & 1))) << 4) + 8 * (p & 1), wx = 32u;
        const unsigned xlo = smb + LDS_X + ch * 128 + ((rw ^ (ch >> 1)) << 4), xhi = xlo + 3 * 2048;
        f32x4 acc[12];
        GFrag fa, fb;
        unsigned wsl = 2, xsl = 0, wa0, wa1, xa0, xa1, xa2, xa3;
        GADDR();
        GSTEP(3, true, fa, fb, false, false);
        GSTEP(3, true, fb, fa, true, true);
        if (w == 0) TSTAMP(7);
#pragma unroll 1
        for (int i = 2; i < 16; i += 2) { GSTEP(3, true, fa, fb, true, false); GSTEP(3, true, fb, fa, true, false); }
        asm volatile("s_nop 7");
        asm volatile("s_waitcnt vmcnt(0)" : "+v"(eraw), "+v"(cpf) :: "memory");
        epoch = (((u64)__builtin_amdgcn_readfirstlane((unsigned)(eraw >> 32)) << 32) | (unsigned)__builtin_amdgcn_readfirstlane((unsigned)eraw)) + 1ull;
        WAIT_LGKM(0);
        BAR();
        const float cs = 0.08838834764831845f * 1.4426950408889634f;
        v2u qp[12];
        unsigned qa0 = smb + LDS_X + ch * 256 + (((4 * w + (rw >> 1)) ^ ch) << 4) + 8 * (rw & 1), qa1 = qa0 ^ 32u;
        PIN(qa0); PIN(qa1);
#define QPK(n) do { qp[n] = v2u{pk2s(acc[n][0], acc[n][1], cs), pk2s(acc[n][2], acc[n][3], cs)}; PIN(qp[n]); } while (0)
#define QST(n) (*(LAS v2u*)((lptr)(size_t)(((n) & 1) ? qa1 : qa0) + ((n) >> 1) * 4096) = qp[n])
        SCHED0();
#pragma unroll
        for (int n = 0; n < 12; ++n) {
            GM(fb, 2 * n, false); SCHED0();
            if (n >= 3) QST(n - 3);
            SCHED0();
            GM(fb, 2 * n + 1, false); SCHED0();
            if (n >= 2) QPK(n - 2);
            SCHED0();
        }
        QST(9); QPK(10); QPK(11); SCHED0(); QST(10); QST(11);
        if (w == 3) { lds_st(sm, M_EPOCH, (unsigned)epoch); lds_st(sm, M_EPOCH + 1, (unsigned)(epoch >> 32)); }
        if (w == 0) TSTAMP(1);
    }
    if (w == 3) { WAIT_LGKM(0); BAR();
    }
    else {
        const unsigned kbase = 2048 * (r >> 3) + 64 * (r & 7) + 16 * (hh ^ ((r >> 2) & 3));
        const unsigned vbase = 64 * (4 * hh + q4) + 16 * ((2 * blk + (p >> 1)) ^ hh) + 8 * (p & 1);
        bf16x8 qf[8], kf[16]; s16x4 vlo[16], vhi[16]; v4u pw[4];
        f32x16 o[4], sa0, sa1, sb0, sb1, negm;
        float lsum = 0.f, ls2 = 0.f, xe = 0.f, xp = 0.f;
        unsigned fw = cls ? (PARK ? 7u : 4u) : 0u, ka, kb, va, vb;
#define SLOT(t) (((unsigned)(SLOT_TAB >> (4u * (t))) & 15u) * WSLOT)
#define SET_K(t) do { ka = smb + SLOT(t) + kbase; kb = ka ^ 32u; PIN(ka); PIN(kb); } while (0)
#define SET_V(t) do { va = smb + SLOT(t) + vbase; vb = va ^ 32u; PIN(va); PIN(vb); } while (0)
#define KRD(i) (kf[i] = *(const LAS bf16x8*)((lptr)(size_t)((((i) >> 1) & 1) ? kb : ka) + ((i) >> 2) * 512 + ((i) & 1) * 8192))
#define VRD(i) do { vlo[i] = __builtin_amdgcn_ds_read_tr16_b64_v4i16((LAS s16x4*)((lptr)(size_t)va + ((i) & 3) * 512 + ((i) >> 2) * 4096));     \
                    vhi[i] = __builtin_amdgcn_ds_read_tr16_b64_v4i16((LAS s16x4*)((lptr)(size_t)vb + ((i) & 3) * 512 + ((i) >> 2) * 4096 + 2048)); } while (0)
#define PV(i) (o[(i) & 3] = MFMA32(__builtin_shufflevector(vlo[i], vhi[i], 0, 1, 2, 3, 4, 5, 6, 7), __builtin_bit_cast(bf16x8, pw[(i) >> 2]), o[(i) & 3]))
#define SV(N0, N1, e) (((e) < 16) ? N0[(e) & 15] : N1[(e) & 15])
#define USE(x) do { asm volatile("" :: "v"(x)); SCHED0(); } while (0)
#define FIN1(e) do { if ((e) & 1) { ls2 += xp; unsigned u_ = pk2(xe, xp); PIN(u_); pw[(e) >> 3][((e) >> 1) & 3] = u_; PIN(ls2); } else { lsum += xp; xe = xp; PIN(lsum); } } while (0)
#define EXP1(S0, S1, e) do { float x_ = __builtin_amdgcn_exp2f(SV(S0, S1, e)); PIN(x_); if ((e) > 0) FIN1((e) - 1); xp = x_; } while (0)
#define XHALF(mx) do { auto r2_ = __builtin_amdgcn_permlane32_swap(__float_as_uint(mx), __float_as_uint(mx), false, false); mx = max2(__uint_as_float(r2_[0]), __uint_as_float(r2_[1])); } while (0)
#define NEED(t) do { if (__builtin_expect(fw <= (t), 0)) { fw = lds_full(sm); lds_st(sm, M_PROG + w, (t) + 1u); wait_full(sm, (t) + 1u, fw); } } while (0)
        SET_K(0u);
        if (cls) {
#pragma unroll
            for (int i = 0; i < 16; ++i) KRD(i);
        }
        unsigned qb0 = smb + LDS_X + w * 8192 + r * 256 + ((hh ^ (r & 15)) << 4), qad[8];
#pragma unroll
        for (int ks = 0; ks < 8; ++ks) { qad[ks] = qb0 ^ (ks << 5); PIN(qad[ks]); }
#pragma unroll
        for (int c = 0; c < 4; ++c) { o[c] = f32x16{}; PIN(o[c]); }
        WAIT_LGKM(0);
        BAR();
        if (w == 0) TSTAMP(2);
#pragma unroll
        for (int ks = 0; ks < 8; ++ks) qf[ks] = *(const LAS bf16x8*)(size_t)qad[ks];
        if (!cls) {
            wait_full(sm, 1u, fw);
#pragma unroll
            for (int i = 0; i < 16; ++i) KRD((2 * i) % 16 + (i >> 3));
        }
        unsigned tk = 1u;
        float mx;
        SCHED0();
#pragma unroll
        for (int i = 0; i < 8; ++i) { sa0 = MFMA32(kf[2 * i], qf[i], i == 0 ? f32x16{} : sa0); SCHED0(); }
        lds_st(sm, M_PROG + w, cls ? 2u : 1u);
#pragma unroll
        for (int i = 0; i < 8; ++i) {
            sa1 = MFMA32(kf[2 * i + 1], qf[i], i == 0 ? f32x16{} : sa1); SCHED0();
            if (i >= 2 && i < 6) { mx = i == 2 ? max2(sa0[0], sa0[1]) : max3(mx, sa0[4 * i - 8], sa0[4 * i - 7]); mx = max3(mx, sa0[4 * i - 6], sa0[4 * i - 5]); PIN(mx); }
            SCHED0();
        }
        lds_st(sm, M_PROG + w, 2u);
        wait_full(sm, 2u, fw);
        SET_K(1u);
#pragma unroll
        for (int i = 0; i < 7; ++i) KRD(i);
        SCHED0();
        { float m2 = max3(sa1[0], sa1[1], sa1[2]);
#pragma unroll
          for (int e = 3; e < 9; e += 2) { m2 = max3(m2, sa1[e], sa1[e + 1]); mx = max3(mx, sa1[e + 6], sa1[e + 7]); }
          mx = max3(mx, m2, sa1[15]);
          XHALF(mx);
          sa0 -= mx; sa1 -= mx;
#pragma unroll
          for (int i = 0; i < 16; ++i) negm[i] = -mx; }
        PIN(negm);
        SCHED0();
#define STEP(S0, S1, N0, N1) do { v2u fv_; if (STAMP > 1 && w == 1) TSTAMP(54 + (tk >> 1)); \
        _Pragma("unroll") for (int i = 0; i < 16; ++i) { \
            if (!(i & 1)) USE(kf[i + 1]); \
            if (i == 0) N0 = MFMA32(kf[0], qf[0], negm); else if (i == 1) N1 = MFMA32(kf[1], qf[0], negm); \
            else if (i & 1) N1 = MFMA32(kf[i], qf[i >> 1], N1); else N0 = MFMA32(kf[i], qf[i >> 1], N0); \
            SCHED0(); \
            if (i < 9) KRD(i + 7); \
            if (i == 4) fv_ = *(const volatile LAS v2u*)(sm + LDS_M + 4 * M_FULL); \
            if (i == 9) { const unsigned f_ = __builtin_amdgcn_readfirstlane(umin(fv_[0], fv_[1])); fw = fw > f_ ? fw : f_; NEED(tk + 1u); SET_V(tk + 1u); } \
            if (i >= 9) VRD(i - 9); \
            SCHED0(); \
            if (i < 8) { EXP1(S0, S1, i + (i >> 1)); if (i & 1) EXP1(S0, S1, i + (i >> 1) + 1); } else EXP1(S0, S1, i + 4); \
            SCHED0(); } \
        float mx_; \
        _Pragma("unroll") for (int i = 0; i < 16; ++i) { \
            if (!(i & 1)) USE(vhi[i + 1]); \
            PV(i); \
            SCHED0(); \
            if (i < 9) VRD(i + 7); \
            if (i == 9) { if (tk < 13u) NEED(tk + 2u); SET_K(tk + 2u); }     \
            if (i >= 9) KRD(i - 9); \
            SCHED0(); \
            if (i < 10) EXP1(S0, S1, 20 + i); \
            if (i == 10) { EXP1(S0, S1, 30); EXP1(S0, S1, 31); FIN1(31); } \
            if (i == 11) { mx_ = max2(N0[0], N0[1]); mx_ = max3(mx_, N0[2], N0[3]); mx_ = max3(mx_, N0[4], N0[5]); mx_ = max3(mx_, N0[6], N0[7]); PIN(mx_); } \
            if (i >= 12 && i < 15) { _Pragma("unroll") for (int e = 8 * (i - 11); e < 8 * (i - 10); e += 2) mx_ = max3(mx_, SV(N0, N1, e), SV(N0, N1, e + 1)); PIN(mx_); } \
            if (i == 13) { S0 = N0; PIN(S0); } \
            if (i == 15) { S1 = N1; PIN(S1); } \
            SCHED0(); } \
        tk += 2u; \
        lds_st(sm, M_PROG + w, tk + 1u); \
        if (__builtin_expect(__builtin_amdgcn_ballot_w64(mx_ > THR) != 0ull, 0)) {     \
            XHALF(mx_); \
            const float dl_ = max2(mx_, 0.f), al_ = __builtin_amdgcn_exp2f(-dl_); \
            lsum *= al_; ls2 *= al_; \
            _Pragma("unroll") for (int i = 0; i < 16; ++i) { S0[i] -= dl_; S1[i] -= dl_; negm[i] -= dl_; o[0][i] *= al_; o[1][i] *= al_; o[2][i] *= al_; o[3][i] *= al_; } } \
        SCHED0(); } while (0)
#pragma unroll 1
        while (tk < 15u) STEP(sa0, sa1, sb0, sb1);
        const unsigned pgv = *(const volatile LAS unsigned*)(sm + LDS_M + 4 * (M_PROG + (lane & 3)));
#pragma unroll
        for (int e = 0; e < 6; ++e) EXP1(sa0, sa1, e);
        if (w == 0) TSTAMP(13);
        wait_full(sm, 16u, fw);
        SET_V(15u);
        SCHED0();
#define TI(i) ((i) < 8 ? (i) : 8 + 4 * ((i) & 1) + (((i) - 8) >> 1))
#pragma unroll
        for (int i = 0; i < 7; ++i) VRD(TI(i));
#pragma unroll
        for (int e = 6; e < 16; ++e) EXP1(sa0, sa1, e);
        if (__builtin_amdgcn_ballot_w64(pgv < 10u) != 0ull) wait_prog(sm, 10u, lane);
        const int row = 32 * w + r;
        const unsigned oa = smb + LDS_X + row * 128 + 8 * hh + (((row >> 1) & 7) << 4);
        unsigned oad[8];
        v2u op[16];
        float inv;
#define OPK(i) do { op[i] = v2u{pk2s(o[(i) >> 2][4 * ((i) & 3)], o[(i) >> 2][4 * ((i) & 3) + 1], inv), pk2s(o[(i) >> 2][4 * ((i) & 3) + 2], o[(i) >> 2][4 * ((i) & 3) + 3], inv)}; PIN(op[i]); } while (0)
#define OST(i) (*(LAS v2u*)((lptr)(size_t)oad[(i) & 7] + ((i) >> 3) * XSLOT) = op[i])
        SCHED0();
#pragma unroll
        for (int i = 0; i < 16; ++i) {
            if (!(i & 1)) USE(vhi[TI(i + 1)]);
            PV(TI(i));
            SCHED0();
            if (i < 9) VRD(TI(i + 7));
            if (i >= 12) OST(i - 12);
            SCHED0();
            if (i < 8) { EXP1(sa0, sa1, 16 + 2 * i); EXP1(sa0, sa1, 17 + 2 * i); }
            if (i == 7) FIN1(31);
            if (i == 8 || i == 10) { _Pragma("unroll") for (int g = 2 * i - 16; g < 2 * i - 12; ++g) { oad[g] = oa ^ (g << 4); PIN(oad[g]); } }
            if (i == 9) { lsum += ls2; auto r2 = __builtin_amdgcn_permlane32_swap(__float_as_uint(lsum), __float_as_uint(lsum), false, false); inv = __builtin_amdgcn_rcpf(__uint_as_float(r2[0]) + __uint_as_float(r2[1])); PIN(inv); }
            if (i >= 11) OPK(i - 11);
            SCHED0();
        }
        lds_st(sm, M_PROG + w, 17u);
        if (w == 0) TSTAMP(3);
        if (w == 2) TSTAMP(14);
        OPK(5); OPK(6); OPK(7); OPK(8); SCHED0(); OST(4); OST(5); OST(6); OST(7); SCHED0();
        OPK(9); OPK(10); OPK(11); OPK(12); SCHED0(); OST(8); OST(9); OST(10); OST(11); SCHED0();
        OPK(13); OPK(14); OPK(15); SCHED0(); OST(12); OST(13); OST(14); OST(15);
    }
    {
        const unsigned woff = smb + LDS_W + (8 * rw + q4) * 256 + (((8 * (hg >> 1) + 2 * w + (p >> 1)) ^ (2 * q4 + 8 * (rw & 1))) << 4) + 8 * (p & 1), wx = 128u;
        const unsigned xb = smb + LDS_X + ch * 128 + ((rw ^ (ch >> 1)) << 4), xlo = xb + (hg & 1) * 3 * 2048, xhi = xb + ((hg & 1) ^ 1) * 3 * 2048;
        f32x4 acc[12];
        GFrag fa, fb;
        unsigned wsl = 3, xsl = 0, wa0, wa1, xa0, xa1, xa2, xa3;
        GADDR();
        WAIT_LGKM(0);
        BAR();
        {
            const rsrc_t sO = __builtin_amdgcn_make_buffer_rsrc((void*)(ws + WS_O + (size_t)bh * 24576), 0, 24576, 0x00020000);
            v4u t[6];
#pragma unroll
            for (int i = 0; i < 6; ++i) t[i] = *(const LAS v4u*)(sm + LDS_X + w * 6144 + i * 1024 + lane * 16);
#pragma unroll
            for (int i = 0; i < 6; ++i) __builtin_amdgcn_raw_buffer_store_b128(t[i], sO, lane * 16 + (i & 3) * 1024, w * 6144 + (i >> 2) * 4096, 16);
        }
        if (w == 0) TSTAMP(16);
        const unsigned okw = *(const volatile LAS unsigned*)sm;
        GSTEP(7, false, fa, fb, false, false);
        GSTEP(7, false, fb, fa, true, true);
        bool pend = true;
        const bool lazy = cls && __builtin_amdgcn_readfirstlane(okw) != 0;
#define TRY_FLAG(force) do { if (pend) { unsigned st_; asm volatile("s_getreg_b32 %0, hwreg(HW_REG_IB_STS, 0, 4)" : "=s"(st_) :: "memory"); \
            if ((force) || st_ == 0u) { WAIT_VM(0); pend = false; if (lane == 0) put_flag((GAS u64*)(ws + WS_FLAG1 + (size_t)(b * 4 + hg) * 256) + hl * 4 + w, epoch); if (w == 0) TSTAMP(12); } } } while (0)
#pragma unroll 1
        for (int i = 2; i < 16; i += 2) { TRY_FLAG(!lazy || i >= 4); GSTEP(7, true, fa, fb, true, false); TRY_FLAG(false); GSTEP(7, true, fb, fa, true, false); }
        asm volatile("s_nop 7");
        const rsrc_t rP = __builtin_amdgcn_make_buffer_rsrc((void*)(ws + WS_PART + ((size_t)(b * 32 + hl) * 4 + w) * 12288), 0, 25 * 4 * 12288, 0x00020000);
        GAS u64* f2 = (GAS u64*)(ws + WS_FLAG2 + ((size_t)(b * 32 + hl) * 4 + w) * 64);
#define AT(j, k) acc[2 * (3 * ((j) & 1) + (k)) + ((j) >> 1)]
#define PST(j) do { const unsigned so_ = (hg ^ (j)) * (8 * 4 * 12288) + hg * 3072; \
            __builtin_amdgcn_raw_buffer_store_b128(v4u{pk2(AT(j, 0)[0], AT(j, 0)[1]), pk2(AT(j, 0)[2], AT(j, 0)[3]), pk2(AT(j, 1)[0], AT(j, 1)[1]), pk2(AT(j, 1)[2], AT(j, 1)[3])}, rP, lane * 16, so_, 16); \
            __builtin_amdgcn_raw_buffer_store_b64(v2u{pk2(AT(j, 2)[0], AT(j, 2)[1]), pk2(AT(j, 2)[2], AT(j, 2)[3])}, rP, lane * 8 + 1024, so_, 16); } while (0)
        SCHED0();
#pragma unroll
        for (int e = 0; e < 12; ++e) {
            const int j = e < 3 ? 2 : e < 6 ? 1 : e < 9 ? 3 : 0, n = 2 * (3 * (j & 1) + e % 3) + (j >> 1);
            GM(fb, 2 * n, false); GM(fb, 2 * n + 1, false); SCHED0();
            if (e == 4) PST(2);
            if (e == 7) PST(1);
            if (e == 10) PST(3);
            SCHED0();
        }
        if (w == 0) TSTAMP(4);
        WAIT_VM(0);
        if (lane < 4 && lane != hg) put_flag(f2 + lane * (8 * 4 * 8) + hg, epoch);
        if (w == 0) TSTAMP(15);
        wait_flags(f2 + hg * (8 * 4 * 8) + ((lane & 3) == hg ? ((hg + 1) & 3) : (lane & 3)), epoch);
        if (w == 0) TSTAMP(5);
        v4u pt[3]; v2u pc[3];
#pragma unroll
        for (int i = 0; i < 3; ++i) {
            const unsigned so = hg * (8 * 4 * 12288) + (hg ^ (i + 1)) * 3072;
            pt[i] = __builtin_amdgcn_raw_buffer_load_b128(rP, lane * 16, so, 16);
            pc[i] = __builtin_amdgcn_raw_buffer_load_b64(rP, lane * 8 + 1024, so, 16);
        }
        char* orow = outg + (size_t)(b * 96 + 48 * (hg & 1) + ch) * 2048 + (nt * 128 + 64 * (hg >> 1) + 16 * w + 4 * rw) * 2;
#pragma unroll
        for (int k = 0; k < 3; ++k) {
            f32x4 v = AT(0, k);
#pragma unroll
            for (int i = 0; i < 3; ++i) { const unsigned u0 = k < 2 ? pt[i][2 * k] : pc[i][0], u1 = k < 2 ? pt[i][2 * k + 1] : pc[i][1];
                v += f32x4{__uint_as_float(u0 << 16), __uint_as_float(u0 & 0xffff0000u), __uint_as_float(u1 << 16), __uint_as_float(u1 & 0xffff0000u)}; }
            *(GAS v2u*)(orow + (size_t)k * 16 * 2048) = v2u{pk2(v[0], v[1]), pk2(v[2], v[3])};
        }
        if (hg == 0 && hl == 0 && w == 0 && lane == 0) __hip_atomic_store((GAS u64*)(ws + WS_EPOCH + b * 256), epoch, RLX_AGENT);
        if (w == 0 && READOUT > 0) { WAIT_VM(0); TSTAMP(6); const u64 n_ = __builtin_amdgcn_s_memrealtime(); while (__builtin_amdgcn_s_memrealtime() < n_ + (rt1 - rt0)) __builtin_amdgcn_s_sleep(1); }
    }
}

extern "C" void kernel_launch(void* const* d_in, const int* in_sizes, int n_in, void* d_out, int out_size, void* d_ws, size_t ws_size, hipStream_t stream) {
    mha_fused<<<256, 512, 0, stream>>>((const char*)d_in[0], (const char*)d_in[1], (const char*)d_in[2], (const char*)d_in[3], (const char*)d_in[4], (char*)d_out, (char*)d_ws);
}
```

```cpp
#include <hip/hip_runtime.h>
#include <hip/hip_bf16.h>

typedef unsigned v4u __attribute__((ext_vector_type(4)));
typedef unsigned v2u __attribute__((ext_vector_type(2)));
typedef short bf16x8 __attribute__((ext_vector_type(8)));
typedef short s16x4 __attribute__((ext_vector_type(4)));
typedef float f32x16 __attribute__((ext_vector_type(16)));
typedef float f32x4 __attribute__((ext_vector_type(4)));
typedef float f32x2 __attribute__((ext_vector_type(2)));
typedef __bf16 bf16x2 __attribute__((ext_vector_type(2)));
typedef unsigned long long u64;
typedef __amdgpu_buffer_rsrc_t rsrc_t;
#define LAS __attribute__((address_space(3)))
#define GAS __attribute__((address_space(1)))
#define DI __device__ __forceinline__
typedef LAS char* lptr;

#define POL_KV " nt"
#define THR 8.f
#define POS_B 3
#define PBF 2
#define WOTHR 8
#define PSLEEP 1
#define PRETOUCH 0
#define CODE_BYTES 28672
#define HSLEEP 4
#define ADELAY 40
#define FSWAP 0
#define ORDA 0x73516240u
#define ORDB 0x62473510u
#define PARK 1
#define STAMP 0

constexpr int WSLOT = 16384, XSLOT = 12288;
constexpr int LDS_W = 0, LDS_X = 7 * WSLOT, LDS_BYTES = LDS_X + 4 * XSLOT;
constexpr int LDS_M = LDS_BYTES - 256;
constexpr u64 SLOT_TAB = 0x2106543872106543ull;
constexpr int M_FULL = 0, M_EPOCH = 2, M_PROG = 8;
constexpr size_t WS_EPOCH = 0;
constexpr size_t WS_FLAG1 = 4096;
constexpr size_t WS_FLAG2 = WS_FLAG1 + 32 * 256;
constexpr size_t WS_O = 131072;
constexpr size_t WS_PART = WS_O + 256 * 24576;
constexpr size_t WS_STAMP = WS_PART + 256 * 4 * 12288;
constexpr unsigned SPIN_CAP = 1u << 18;

DI unsigned pk2(float a, float b) { f32x2 v = {a, b}; return __builtin_bit_cast(unsigned, __builtin_convertvector(v, bf16x2)); }
DI unsigned pk2s(float a, float b, float s) { f32x2 v = {a, b}; v *= s; return __builtin_bit_cast(unsigned, __builtin_convertvector(v, bf16x2)); }
DI float max2(float a, float b) { return __builtin_elementwise_maximum(a, b); }
DI float max3(float a, float b, float c) { return max2(max2(a, b), c); }
DI unsigned umin(unsigned a, unsigned b) { return a < b ? a : b; }
DI unsigned lds_ld(lptr sm, int word) {
    unsigned r; const unsigned a = (unsigned)(size_t)(sm + LDS_M) + 4u * (unsigned)word;
    asm volatile("ds_read_b32 %0, %1\n\ts_waitcnt lgkmcnt(0)" : "=v"(r) : "v"(a) : "memory"); return r; }
DI v2u lds_ld2(lptr sm, int word) {
    v2u r; const unsigned a = (unsigned)(size_t)(sm + LDS_M) + 4u * (unsigned)word;
    asm volatile("ds_read_b64 %0, %1\n\ts_waitcnt lgkmcnt(0)" : "=v"(r) : "v"(a) : "memory"); return r; }
DI unsigned lds_full(lptr sm) { const v2u r = lds_ld2(sm, M_FULL); return __builtin_amdgcn_readfirstlane(umin(r[0], r[1])); }
DI void lds_st(lptr sm, int word, unsigned v) {
    const unsigned a = (unsigned)(size_t)(sm + LDS_M) + 4u * (unsigned)word;
    asm volatile("ds_write_b32 %0, %1" :: "v"(a), "v"(v) : "memory"); }
DI void lds_max(lptr sm, int word, unsigned v, int lane) {
    const unsigned a = (unsigned)(size_t)(sm + LDS_M) + 4u * (unsigned)word;
    if (lane == 0) asm volatile("ds_max_u32 %0, %1" :: "v"(a), "v"(v) : "memory"); }
#define CFENCE() asm volatile("" ::: "memory")
#define SCHED0() __builtin_amdgcn_sched_barrier(0)
#define WAIT_LGKM(n) do { __builtin_amdgcn_s_waitcnt(0xC07F | ((n) << 8)); asm volatile("" ::: "memory"); } while (0)
#define WAIT_VM(n) asm volatile("s_waitcnt vmcnt(%0)" :: "i"(n) : "memory")
#define BAR() asm volatile("s_barrier" ::: "memory")
#define PIN(x) asm volatile("" : "+v"(x))
#define MFMA32(a, b, c) __builtin_amdgcn_mfma_f32_32x32x16_bf16(a, b, c, 0, 0, 0)
#define RLX_AGENT __ATOMIC_RELAXED, __HIP_MEMORY_SCOPE_AGENT
DI void put_flag(GAS u64* p, u64 v) { if (FSWAP) (void)__hip_atomic_exchange(p, v, RLX_AGENT); else __hip_atomic_store(p, v, RLX_AGENT); }

DI void wait_full(lptr sm, unsigned need, unsigned& cur) {
    unsigned spins = 0;
#pragma nounroll
    while (__builtin_expect(cur < need, 0)) {
        cur = lds_full(sm);
        if (cur >= need || ++spins > SPIN_CAP) break;
        __builtin_amdgcn_s_sleep(1);
    }
    CFENCE();
}
DI void wait_prog(lptr sm, unsigned need, int lane) {
    unsigned spins = 0;
#pragma nounroll
    while (__builtin_amdgcn_ballot_w64(lds_ld(sm, M_PROG + (lane & 3)) < need) != 0ull && ++spins <= SPIN_CAP) __builtin_amdgcn_s_sleep(6);
    CFENCE();
}
DI void wait_flags(GAS u64* fl, u64 epoch) {
    unsigned spins = 0;
#pragma nounroll
    for (;;) {
        const u64 f = __hip_atomic_load(fl, RLX_AGENT);
        if (__builtin_amdgcn_ballot_w64(f != epoch) == 0ull || ++spins > SPIN_CAP) break;
        __builtin_amdgcn_s_sleep(PSLEEP);
    }
    CFENCE();
}
#define TSTAMP(k) do {} while (0)
#define TDUMP() do {} while (0)

DI v4u mk_srd(const void* p, unsigned bytes) { const u64 a = (u64)p; return v4u{(unsigned)a, (unsigned)(a >> 32) & 0xffffu, bytes, 0x00020000u}; }
#define DMA4(POL, srd, dst, v, soff) asm volatile("s_mov_b32 m0, %0\n\ts_nop 0\n\tbuffer_load_dwordx4 %3, %1, %2 offen" POL " lds\n\tbuffer_load_dwordx4 %4, %1, %2 offen offset:1024" POL " lds\n\t" \
        "buffer_load_dwordx4 %5, %1, %2 offen offset:2048" POL " lds\n\tbuffer_load_dwordx4 %6, %1, %2 offen offset:3072" POL " lds" \
        :: "s"((unsigned)(dst)), "s"(srd), "s"((unsigned)(soff)), "v"(v[0]), "v"(v[1]), "v"(v[2]), "v"(v[3]) : "memory")
#define DMA3(POL, srd, dst, v, soff) asm volatile("s_mov_b32 m0, %0\n\ts_nop 0\n\tbuffer_load_dwordx4 %3, %1, %2 offen" POL " lds\n\tbuffer_load_dwordx4 %4, %1, %2 offen offset:1024" POL " lds\n\t" \
        "buffer_load_dwordx4 %5, %1, %2 offen offset:2048" POL " lds" \
        :: "s"((unsigned)(dst)), "s"(srd), "s"((unsigned)(soff)), "v"(v[0]), "v"(v[1]), "v"(v[2]) : "memory")

struct GFrag { bf16x8 xa[12]; s16x4 wl[4], wh[4]; };
#define GM(F, m, Z) (acc[(m) >> 1] = __builtin_amdgcn_mfma_f32_16x16x32_bf16(__builtin_shufflevector(F.wl[(m) & 3], F.wh[(m) & 3], 0, 1, 2, 3, 4, 5, 6, 7), F.xa[2 * ((m) >> 2) + ((m) & 1)], \
                                                                             (Z) && !((m) & 1) ? f32x4{} : acc[(m) >> 1], 0, 0, 0))
#define GMA(F, m, Z) do { const bf16x8 a_ = __builtin_shufflevector(F.wl[(m) & 3], F.wh[(m) & 3], 0, 1, 2, 3, 4, 5, 6, 7); \
        if ((Z) && !((m) & 1)) asm volatile("v_mfma_f32_16x16x32_bf16 %0, %1, %2, 0" : "=&v"(acc[(m) >> 1]) : "v"(a_), "v"(F.xa[2 * ((m) >> 2) + ((m) & 1)])); \
        else asm volatile("v_mfma_f32_16x16x32_bf16 %0, %1, %2, %0" : "+v"(acc[(m) >> 1]) : "v"(a_), "v"(F.xa[2 * ((m) >> 2) + ((m) & 1)])); } while (0)
#define GADDR() do { wa0 = wsl * WSLOT + woff; wa1 = wa0 ^ wx; xa0 = xsl * XSLOT + xlo; xa1 = xa0 ^ 64u; xa2 = xsl * XSLOT + xhi; xa3 = xa2 ^ 64u; PIN(wa0); PIN(wa1); PIN(xa0); PIN(xa1); PIN(xa2); PIN(xa3); } while (0)
#define GSTEP(RING, DOBAR, FCUR, FPREV, HASPREV, Z) do { \
        if (DOBAR) BAR(); \
        SCHED0(); \
        _Pragma("unroll") for (int m = 0; m < 24; ++m) { \
            if (HASPREV) GMA(FPREV, m, Z); \
            SCHED0(); \
            if (m < 8) { const lptr wt = (lptr)(size_t)((m & 4) ? wa1 : wa0) + ((m >> 1) & 1) * 32 * 256 + (m & 1) * 4 * 256; \
                if (m & 1) FCUR.wh[m >> 1] = __builtin_amdgcn_ds_read_tr16_b64_v4i16((LAS s16x4*)wt); else FCUR.wl[m >> 1] = __builtin_amdgcn_ds_read_tr16_b64_v4i16((LAS s16x4*)wt); } \
            else if (m < 20) FCUR.xa[m - 8] = *(const LAS bf16x8*)((lptr)(size_t)(m < 14 ? ((m & 1) ? xa1 : xa0) : ((m & 1) ? xa3 : xa2)) + (((m - 8) >> 1) % 3) * 2048); \
            else if (m == 20) { wsl = (wsl + 1 == (RING)) ? 0 : wsl + 1; xsl = (xsl + 1) & 3; wa0 = wsl * WSLOT + woff; wa1 = wa0 ^ wx; PIN(wa0); PIN(wa1); } \
            else if (m == 21) { xa0 = xsl * XSLOT + xlo; xa1 = xa0 ^ 64u; PIN(xa0); PIN(xa1); } \
            else if (m == 22) { xa2 = xsl * XSLOT + xhi; xa3 = xa2 ^ 64u; PIN(xa2); PIN(xa3); } \
            SCHED0(); } \
        WAIT_LGKM(12); } while (0)

__global__ __launch_bounds__(512, 2) void mha_fused(const char* __restrict__ xg, const char* __restrict__ kg, const char* __restrict__ vg,
                                                    const char* __restrict__ wqg, const char* __restrict__ wog, char* __restrict__ outg, char* __restrict__ ws) {
    __shared__ __attribute__((aligned(16))) char smem[LDS_BYTES];
    const lptr sm = (lptr)smem;
    const unsigned smb = (unsigned)(size_t)sm;
    const int tid = threadIdx.x, lane = tid & 63, w = __builtin_amdgcn_readfirstlane(tid >> 6);
    const int wg = blockIdx.x, xcd = wg & 7, idx = wg >> 3;
    const int hg = xcd >> 1, b = idx >> 2, hl = 4 * (xcd & 1) + (idx & 3), h = 8 * hg + hl;
    const int nt = (hl & 4) + ((hl + b) & 3);
    const int bh = b * 32 + h;
    const int cls = hl & 1;
    const int rw = lane >> 4, ch = lane & 15, lw = w & 3;
    const int r = lane & 31, hh = lane >> 5, blk = (lane >> 4) & 1, q4 = (lane & 15) >> 2, p = lane & 3;
    u64 epoch = 0;
    u64 code0; asm volatile("s_getpc_b64 %0" : "=s"(code0)); code0 &= ~(u64)255;
    if (w == 0) TSTAMP(0);

#define PT() do { if (w == 3) {     \
        const char* p_ = lane < 32 ? wog + (size_t)hg * 1024 * 2048 + lane * 65536 + nt * 256 \
            : lane < 36 ? ws + WS_O + (size_t)(b * 32 + hg * 8) * 24576 + (lane - 32) * 65280 \
            : lane < 44 ? ws + WS_PART + (size_t)(b * 32 + hl + 8 * ((lane - 36) >> 1)) * 4 * 12288 + (lane & 1) * 49024 \
            : lane < 48 ? outg + (size_t)b * 96 * 2048 + (lane - 44) * 65280 \
            : lane < 56 ? kg + (size_t)bh * 131072 + (lane & 1) * 65536 : vg + (size_t)bh * 131072 + (lane & 1) * 65536; \
        const unsigned v_ = __hip_atomic_load((GAS unsigned*)p_, RLX_AGENT); asm volatile("" :: "v"(v_)); } } while (0)
    if (__builtin_expect(w >= 4, 1)) {
        const int rot = (4 * hl + 2 * b) & 15;
        const v4u rq = mk_srd(wqg + h * 256 + lw * 16 * 8192, 1024 * 8192 - (h * 256 + lw * 16 * 8192));
        const v4u rx = mk_srd(xg + (size_t)(b * 96 + lw * 24) * 2048, 24 * 2048);
        unsigned vq[4], vx[3];
#pragma unroll
        for (int i = 0; i < 4; ++i) vq[i] = (4 * i + rw) * 8192 - i * 1024 + ((ch ^ (2 * rw + 8 * (i >> 1))) << 4);
#pragma unroll
        for (int i = 0; i < 3; ++i) vx[i] = (8 * i + (lane >> 3)) * 2048 - i * 1024 + (((lane & 7) ^ (4 * ((3 * lw + i) & 1) + rw)) << 4);
        const unsigned wdst = smb + LDS_W + lw * 4096, xdst = smb + LDS_X + lw * 3072;
#define P1_ISSUE(i) do { const unsigned kt_ = (unsigned)((i) + rot) & 15u; \
            DMA3("", rx, xdst + ((i) & 3) * XSLOT, vx, kt_ * 128u); \
            DMA4("", rq, wdst + (((i) + 2) % 3) * WSLOT, vq, kt_ * (64u * 8192u)); } while (0)
        P1_ISSUE(0);
        SCHED0();
        __builtin_amdgcn_s_setprio(2);
        if (w == 7) TSTAMP(24);
        P1_ISSUE(1);
        const bool early = w >= 6 && cls == 1;
        const v4u rk = mk_srd(kg + (size_t)bh * 131072 - 1024, 131072 + 1024), rv = mk_srd(vg + (size_t)bh * 131072 - 1024, 131072 + 1024);
        unsigned vk[4];
#pragma unroll
        for (int i = 0; i < 4; ++i) vk[i] = (8 * (i >> 1) + ((lane >> 2) & 7)) * 256 + (i & 1) * 128 + (lane >> 5) * 64 + (((lane & 3) ^ ((2 * (i >> 1) + ((lane >> 4) & 1)) & 3)) << 4) - i * 1024 + 1024;
        const unsigned hw = (w & 1) * 8192;
#define K_ISSUE(slot, j) do { DMA4(POL_KV, rk, smb + LDS_W + (slot) * WSLOT + hw, vk, (j) * 16384 + hw); DMA4(POL_KV, rk, smb + LDS_W + (slot) * WSLOT + hw + 4096, vk, (j) * 16384 + hw + 4096); } while (0)
#define V_ISSUE(slot, j) do { DMA4(POL_KV, rv, smb + LDS_W + (slot) * WSLOT + hw, vk, (j) * 16384 + hw); DMA4(POL_KV, rv, smb + LDS_W + (slot) * WSLOT + hw + 4096, vk, (j) * 16384 + hw + 4096); } while (0)
        v4u park[3][8];
#define PARK_LD4(d, o, srd, v, soff) asm volatile("buffer_load_dwordx4 %0, %6, %4, %5 offen" POL_KV "\n\tbuffer_load_dwordx4 %1, %7, %4, %5 offen offset:1024" POL_KV "\n\t" \
            "buffer_load_dwordx4 %2, %8, %4, %5 offen offset:2048" POL_KV "\n\tbuffer_load_dwordx4 %3, %9, %4, %5 offen offset:3072" POL_KV \
            : "=&v"(d[o]), "=&v"(d[o + 1]), "=&v"(d[o + 2]), "=&v"(d[o + 3]) : "s"(srd), "s"((unsigned)(soff)), "v"(v[0]), "v"(v[1]), "v"(v[2]), "v"(v[3]) : "memory")
#define PARK_LD(n, srd, v, j) do { PARK_LD4(park[n], 0, srd, v, (j) * 16384 + hw); PARK_LD4(park[n], 4, srd, v, (j) * 16384 + hw + 4096); } while (0)
#define PARK_WR(n) do { _Pragma("unroll") for (int i_ = 0; i_ < 8; ++i_) *(LAS v4u*)(sm + LDS_W + (n) * WSLOT + hw + i_ * 1024 + lane * 16) = park[n][i_]; } while (0)
        WAIT_VM(7); BAR();
        P1_ISSUE(2);
#pragma unroll
        for (int i = 1; i < 14; ++i) {
            if ((i == POS_B + 1 || i == POS_B + 2) && early) WAIT_VM(PARK ? 63 : 39); else WAIT_VM(7);
            BAR();
            P1_ISSUE(i + 2);
            if (i == POS_B && early) { K_ISSUE(3, 0); K_ISSUE(4, 1); V_ISSUE(5, 0); K_ISSUE(6, 2);
                if (PARK) { WAIT_VM(32); PARK_LD(0, rv, vk, 1); PARK_LD(1, rk, vk, 3); PARK_LD(2, rv, vk, 2); } }
        }
        const unsigned ord = cls ? ORDB : ORDA;
#define KT(i) ((((unsigned)hl + (ord >> (4u * ((unsigned)(i) >> 1)))) & 7u) * 2u + ((unsigned)(i) & 1u))
        const v4u ro = mk_srd(wog + (size_t)hg * 1024 * 2048 + nt * 256, 1024 * 2048 - nt * 256);
        unsigned vo[4];
#pragma unroll
        for (int i = 0; i < 4; ++i) vo[i] = (4 * i + rw) * 2048 - i * 1024 + ((ch ^ (2 * rw + 8 * (i >> 1))) << 4);
        const v4u rO = mk_srd(ws + WS_O + (size_t)(b * 32 + hg * 8) * 24576, 8 * 24576);
        GAS u64* f1 = (GAS u64*)(ws + WS_FLAG1 + (size_t)(b * 4 + hg) * 256);
        GAS u64* f1a = f1 + (lane & 31);
        unsigned rdy = 0;
#define HEAD_OK(n) (((rdy >> (2u * KT(2 * (n)))) & 15u) == 15u)
#define HEAD_WAIT(n) do { unsigned sp_ = 0; _Pragma("nounroll") while (!HEAD_OK(n) && ++sp_ <= SPIN_CAP) { if (HSLEEP && sp_ > 1) __builtin_amdgcn_s_sleep(HSLEEP); rdy = (unsigned)__builtin_amdgcn_ballot_w64(__hip_atomic_load(f1a, RLX_AGENT) == epoch); } CFENCE(); } while (0)
        if (w < 6) {
            WAIT_VM(7); BAR();
            WAIT_VM(0); BAR();
            BAR();
            if (w == 4 && lane < 16 && lane != M_EPOCH && lane != M_EPOCH + 1) lds_st(sm, lane, lane == M_PROG + 3 ? 1000u : 0u);
            WAIT_LGKM(0);
            BAR();
            if (w == 4) {
                const v2u e = lds_ld2(sm, M_EPOCH); epoch = ((u64)__builtin_amdgcn_readfirstlane(e[1]) << 32) | (unsigned)__builtin_amdgcn_readfirstlane(e[0]);
                unsigned v0[4];
#pragma unroll
                for (int i = 0; i < 4; ++i) v0[i] = lane * 16;
#define O_WHOLE(i) do { _Pragma("unroll") for (int g_ = 0; g_ < 3; ++g_) DMA4(" sc1", rO, smb + LDS_X + (i) * XSLOT + g_ * 4096, v0, KT(i) * (unsigned)XSLOT + g_ * 4096); } while (0)
                bool ok = false; u64 fv1 = 0; PIN(fv1);
                if (cls) {
                    unsigned spins = 0, pv;
#pragma nounroll
                    while (__builtin_amdgcn_ballot_w64(lds_ld(sm, M_PROG + (lane & 3)) < 16u) != 0ull && ++spins <= SPIN_CAP) __builtin_amdgcn_s_sleep(8);
                    asm volatile("global_load_dwordx2 %0, %1, off sc1" : "+v"(fv1) : "v"(f1a) : "memory");
#pragma nounroll
                    for (;;) {
                        unsigned st; asm volatile("s_getreg_b32 %0, hwreg(HW_REG_IB_STS, 0, 4)" : "=s"(st) :: "memory");
                        if (st == 0u) {
                            asm volatile("s_waitcnt vmcnt(0)" : "+v"(fv1) :: "memory");
                            rdy = (unsigned)__builtin_amdgcn_ballot_w64(fv1 == epoch); ok = HEAD_OK(1);
                            if (ok) break;
                            __builtin_amdgcn_s_sleep(4);
                            asm volatile("global_load_dwordx2 %0, %1, off sc1" : "+v"(fv1) : "v"(f1a) : "memory");
                        }
                        __builtin_amdgcn_s_sleep(1);
                        pv = lds_ld(sm, M_PROG + (lane & 3));
                        if (__builtin_amdgcn_ballot_w64(pv == 17u) != 0ull || ++spins > SPIN_CAP) break;
                    }
                    if (ok) O_WHOLE(2);
                    *(volatile LAS unsigned*)sm = (ok && HEAD_OK(2)) ? 1u : 0u;
                    WAIT_LGKM(0);
                }
                BAR();
                if (!ok) {
                    if (ADELAY && !cls) __builtin_amdgcn_s_sleep(ADELAY);
                    asm volatile("s_waitcnt vmcnt(0)" : "+v"(fv1) :: "memory");
                    rdy = (unsigned)__builtin_amdgcn_ballot_w64(fv1 == epoch); HEAD_WAIT(1);
                    O_WHOLE(2);
                }
                TSTAMP(11);
                O_WHOLE(3);
                HEAD_WAIT(2);
                WAIT_VM(12);
            } else BAR();
        } else {
            if (!early) { K_ISSUE(3, 0); WAIT_VM(15); } else WAIT_VM(7);
            BAR();
            if (!early) { K_ISSUE(4, 1); WAIT_VM(16); } else if (PARK) WAIT_VM(0); else { V_ISSUE(0, 1); WAIT_VM(8); }
            BAR();
            if (!early) V_ISSUE(5, 0); else if (!PARK) K_ISSUE(1, 3);
            BAR();
            if (!early) K_ISSUE(6, 2); else if (PARK) { PARK_WR(0); PARK_WR(1); PARK_WR(2); }
            WAIT_LGKM(0);
            BAR();
            const unsigned pa_ = smb + LDS_M + 4u * (M_PROG + (lane & 3)), fa_ = smb + LDS_M + 4u * (M_FULL + (w & 1));
            {
                const unsigned t0 = early ? (PARK ? 7u : 6u) : 4u;
                const u64 kb_ = (u64)(kg + (size_t)bh * 131072 + 16384 - 1024 + hw), vb_ = (u64)(vg + (size_t)bh * 131072 - 1024 + hw);
                unsigned pv, tv;
                asm volatile(
                    "s_mov_b32 s88, %[t0]\n\ts_mov_b64 s[90:91], %[tab]\n\ts_mov_b32 s94, %[nrec]\n\ts_mov_b32 s95, 0x20000\n\ts_mov_b32 s97, %[cap]\n"
                    "1:\n\ts_waitcnt vmcnt(15)\n\ts_sub_i32 s89, s88, 7\n\ts_cmp_eq_u32 s88, 7\n\ts_addc_u32 s89, s89, 0\n"
                    "2:\n\ts_getreg_b32 s96, hwreg(HW_REG_IB_STS, 0, 4)\n\tds_read_b32 %[pv], %[pa]\n\ts_add_u32 s96, s96, 7\n\ts_lshr_b32 s96, s96, 3\n\ts_sub_u32 s96, s88, s96\n\t"
                    "v_mov_b32 %[tv], s96\n\tds_write_b32 %[fa], %[tv]\n\ts_waitcnt lgkmcnt(1)\n\tv_cmp_gt_i32 vcc, s89, %[pv]\n\ts_cbranch_vccz 3f\n\t"
                    "s_sleep 2\n\ts_sub_u32 s97, s97, 1\n\ts_cbranch_scc0 2b\n\ts_mov_b32 s97, 0\n"
                    "3:\n\ts_and_b32 s96, s90, 15\n\ts_lshl_b32 s96, s96, 14\n\ts_add_u32 m0, s96, %[lb]\n\ts_bitcmp1_b32 %[km], s88\n\ts_cselect_b64 s[92:93], %[kb], %[vb]\n\t"
                    "s_sub_u32 s89, s88, 1\n\ts_lshl_b32 s89, s89, 13\n\ts_bitset0_b32 s89, 13\n\t"
                    "buffer_load_dwordx4 %[v0], s[92:95], s89 offen" POL_KV " lds\n\tbuffer_load_dwordx4 %[v1], s[92:95], s89 offen offset:1024" POL_KV " lds\n\t"
                    "buffer_load_dwordx4 %[v2], s[92:95], s89 offen offset:2048" POL_KV " lds\n\tbuffer_load_dwordx4 %[v3], s[92:95], s89 offen offset:3072" POL_KV " lds\n\t"
                    "s_add_u32 m0, m0, 0x1000\n\ts_bitset1_b32 s89, 12\n\t"
                    "buffer_load_dwordx4 %[v0], s[92:95], s89 offen" POL_KV " lds\n\tbuffer_load_dwordx4 %[v1], s[92:95], s89 offen offset:1024" POL_KV " lds\n\t"
                    "buffer_load_dwordx4 %[v2], s[92:95], s89 offen offset:2048" POL_KV " lds\n\tbuffer_load_dwordx4 %[v3], s[92:95], s89 offen offset:3072" POL_KV " lds\n\t"
                    "s_lshr_b64 s[90:91], s[90:91], 4\n\ts_add_u32 s88, s88, 1\n\ts_cmp_lt_u32 s88, 16\n\ts_cbranch_scc1 1b"
                    : [pv] "=&v"(pv), [tv] "=&v"(tv)
                    : [t0] "s"(t0), [tab] "s"(SLOT_TAB >> (4u * t0)), [nrec] "s"(131072u + 1024u - hw), [cap] "s"(SPIN_CAP), [pa] "v"(pa_), [fa] "v"(fa_), [lb] "s"(smb + hw), [km] "s"(0x2AAAu),
                      [kb] "s"(kb_), [vb] "s"(vb_), [v0] "v"(vk[0]), [v1] "v"(vk[1]), [v2] "v"(vk[2]), [v3] "v"(vk[3])
                    : "s88", "s89", "s90", "s91", "s92", "s93", "s94", "s95", "s96", "s97", "vcc", "scc", "memory");
            }
            if (w == 7) TSTAMP(25);
            const int fwd = M_FULL + (w & 1);
            WAIT_VM(16); lds_max(sm, fwd, 14u, lane);
            WAIT_VM(8); lds_st(sm, fwd, 15u);
            WAIT_VM(0); lds_st(sm, fwd, 16u);
            if (w == 7) TSTAMP(22);
#define WO_ISSUE2(i) do { const unsigned d_ = smb + LDS_W + (((i) + 3) % 7) * WSLOT + hw, so_ = (KT(i) * 64u + (w & 1) * 32u) * 2048u; \
            DMA4("", ro, d_, vo, so_); DMA4("", ro, d_ + 4096u, vo, so_ + 16u * 2048u); } while (0)
            wait_prog(sm, 12u, lane); WO_ISSUE2(0); WO_ISSUE2(1);
            wait_prog(sm, 14u, lane); WAIT_VM(8); WO_ISSUE2(2); WAIT_VM(8); WO_ISSUE2(3);
            WAIT_VM(16);
            if (w == 7) TSTAMP(23);
            BAR();
            WAIT_VM(8);
        }
        unsigned vl[3];
#pragma unroll
        for (int i = 0; i < 3; ++i) vl[i] = lane * 16 + lw * 3072;
#define O_ISSUE(i) DMA3(" sc1", rO, xdst + ((i) & 3) * XSLOT, vl, KT(i) * (unsigned)XSLOT)
#define WO_ISSUE(i) DMA4("", ro, wdst + (((i) + 3) % 7) * WSLOT, vo, (KT(i) * 64u + lw * 16u) * 2048u)
        u64 fv = 0; PIN(fv);
#define REFRESH(n) do { if (w == 4 && !HEAD_OK(n)) asm volatile("global_load_dwordx2 %0, %1, off sc1" : "+v"(fv) : "v"(f1a) : "memory"); } while (0)
        BAR();
        O_ISSUE(4); WO_ISSUE(4); REFRESH(3); WO_ISSUE(5); WAIT_VM(11); BAR();
#pragma unroll
        for (int i = 3; i < 13; ++i) {
            if (!(i & 1) && i < 12) REFRESH((i + 4) >> 1);
            O_ISSUE(i + 2); WO_ISSUE(i + 3); WAIT_VM(11);
            if ((i & 1) && w == 4) { PIN(fv); rdy |= (unsigned)__builtin_amdgcn_ballot_w64(fv == epoch); if (i == 5) TSTAMP(20); HEAD_WAIT((i + 3) >> 1); if (i == 5) TSTAMP(21); }
            BAR();
        }
        O_ISSUE(15); WAIT_VM(4 + 3); BAR();
        WAIT_VM(0); BAR();
        TDUMP();
        return;
    }
    {
        u64 eraw; unsigned cpf = 0;
        asm volatile("global_load_dwordx2 %0, %1, off sc1" : "=v"(eraw) : "v"(ws + WS_EPOCH + b * 256) : "memory");
        const unsigned woff = smb + LDS_W + (8 * rw + q4) * 256 + (((4 * w + (p >> 1)) ^ (2 * q4 + 8 * (rw & 1))) << 4) + 8 * (p & 1), wx = 32u;
        const unsigned xlo = smb + LDS_X + ch * 128 + ((rw ^ (ch >> 1)) << 4), xhi = xlo + 3 * 2048;
        f32x4 acc[12];
        GFrag fa, fb;
        unsigned wsl = 2, xsl = 0, wa0, wa1, xa0, xa1, xa2, xa3;
        GADDR();
        GSTEP(3, true, fa, fb, false, false);
        GSTEP(3, true, fb, fa, true, true);
#pragma unroll 1
        for (int i = 2; i < 16; i += 2) { GSTEP(3, true, fa, fb, true, false); GSTEP(3, true, fb, fa, true, false); }
        asm volatile("s_nop 7");
        asm volatile("s_waitcnt vmcnt(0)" : "+v"(eraw), "+v"(cpf) :: "memory");
        epoch = (((u64)__builtin_amdgcn_readfirstlane((unsigned)(eraw >> 32)) << 32) | (unsigned)__builtin_amdgcn_readfirstlane((unsigned)eraw)) + 1ull;
        WAIT_LGKM(0);
        BAR();
        const float cs = 0.08838834764831845f * 1.4426950408889634f;
        v2u qp[12];
        unsigned qa0 = smb + LDS_X + ch * 256 + (((4 * w + (rw >> 1)) ^ ch) << 4) + 8 * (rw & 1), qa1 = qa0 ^ 32u;
        PIN(qa0); PIN(qa1);
#define QPK(n) do { qp[n] = v2u{pk2s(acc[n][0], acc[n][1], cs), pk2s(acc[n][2], acc[n][3], cs)}; PIN(qp[n]); } while (0)
#define QST(n) (*(LAS v2u*)((lptr)(size_t)(((n) & 1) ? qa1 : qa0) + ((n) >> 1) * 4096) = qp[n])
        SCHED0();
#pragma unroll
        for (int n = 0; n < 12; ++n) {
            GM(fb, 2 * n, false); SCHED0();
            if (n >= 3) QST(n - 3);
            SCHED0();
            GM(fb, 2 * n + 1, false); SCHED0();
            if (n >= 2) QPK(n - 2);
            SCHED0();
        }
        QST(9); QPK(10); QPK(11); SCHED0(); QST(10); QST(11);
        if (w == 3) { lds_st(sm, M_EPOCH, (unsigned)epoch); lds_st(sm, M_EPOCH + 1, (unsigned)(epoch >> 32)); }
        if (w == 0) TSTAMP(1);
    }
    if (w == 3) { WAIT_LGKM(0); BAR();
    }
    else {
        const unsigned kbase = 2048 * (r >> 3) + 64 * (r & 7) + 16 * (hh ^ ((r >> 2) & 3));
        const unsigned vbase = 64 * (4 * hh + q4) + 16 * ((2 * blk + (p >> 1)) ^ hh) + 8 * (p & 1);
        bf16x8 qf[8], kf[16]; s16x4 vlo[16], vhi[16]; v4u pw[4];
        f32x16 o[4], sa0, sa1, sb0, sb1, negm;
        float lsum = 0.f, ls2 = 0.f, xe = 0.f, xp = 0.f;
        unsigned fw = cls ? (PARK ? 7u : 4u) : 0u, ka, kb, va, vb;
#define SLOT(t) (((unsigned)(SLOT_TAB >> (4u * (t))) & 15u) * WSLOT)
#define SET_K(t) do { ka = smb + SLOT(t) + kbase; kb = ka ^ 32u; PIN(ka); PIN(kb); } while (0)
#define SET_V(t) do { va = smb + SLOT(t) + vbase; vb = va ^ 32u; PIN(va); PIN(vb); } while (0)
#define KRD(i) (kf[i] = *(const LAS bf16x8*)((lptr)(size_t)((((i) >> 1) & 1) ? kb : ka) + ((i) >> 2) * 512 + ((i) & 1) * 8192))
#define VRD(i) do { vlo[i] = __builtin_amdgcn_ds_read_tr16_b64_v4i16((LAS s16x4*)((lptr)(size_t)va + ((i) & 3) * 512 + ((i) >> 2) * 4096));     \
                    vhi[i] = __builtin_amdgcn_ds_read_tr16_b64_v4i16((LAS s16x4*)((lptr)(size_t)vb + ((i) & 3) * 512 + ((i) >> 2) * 4096 + 2048)); } while (0)
#define PV(i) (o[(i) & 3] = MFMA32(__builtin_shufflevector(vlo[i], vhi[i], 0, 1, 2, 3, 4, 5, 6, 7), __builtin_bit_cast(bf16x8, pw[(i) >> 2]), o[(i) & 3]))
#define SV(N0, N1, e) (((e) < 16) ? N0[(e) & 15] : N1[(e) & 15])
#define USE(x) do { asm volatile("" :: "v"(x)); SCHED0(); } while (0)
#define FIN1(e) do { if ((e) & 1) { ls2 += xp; unsigned u_ = pk2(xe, xp); PIN(u_); pw[(e) >> 3][((e) >> 1) & 3] = u_; PIN(ls2); } else { lsum += xp; xe = xp; PIN(lsum); } } while (0)
#define EXP1(S0, S1, e) do { float x_ = __builtin_amdgcn_exp2f(SV(S0, S1, e)); PIN(x_); if ((e) > 0) FIN1((e) - 1); xp = x_; } while (0)
#define XHALF(mx) do { auto r2_ = __builtin_amdgcn_permlane32_swap(__float_as_uint(mx), __float_as_uint(mx), false, false); mx = max2(__uint_as_float(r2_[0]), __uint_as_float(r2_[1])); } while (0)
#define NEED(t) do { if (__builtin_expect(fw <= (t), 0)) { fw = lds_full(sm); lds_st(sm, M_PROG + w, (t) + 1u); wait_full(sm, (t) + 1u, fw); } } while (0)
        SET_K(0u);
        if (cls) {
#pragma unroll
            for (int i = 0; i < 16; ++i) KRD(i);
        }
        unsigned qb0 = smb + LDS_X + w * 8192 + r * 256 + ((hh ^ (r & 15)) << 4), qad[8];
#pragma unroll
        for (int ks = 0; ks < 8; ++ks) { qad[ks] = qb0 ^ (ks << 5); PIN(qad[ks]); }
#pragma unroll
        for (int c = 0; c < 4; ++c) { o[c] = f32x16{}; PIN(o[c]); }
        WAIT_LGKM(0);
        BAR();
        if (w == 0) TSTAMP(2);
#pragma unroll
        for (int ks = 0; ks < 8; ++ks) qf[ks] = *(const LAS bf16x8*)(size_t)qad[ks];
        if (!cls) {
            wait_full(sm, 1u, fw);
#pragma unroll
            for (int i = 0; i < 16; ++i) KRD((2 * i) % 16 + (i >> 3));
        }
        unsigned tk = 1u;
        float mx;
        SCHED0();
#pragma unroll
        for (int i = 0; i < 8; ++i) { sa0 = MFMA32(kf[2 * i], qf[i], i == 0 ? f32x16{} : sa0); SCHED0(); }
        lds_st(sm, M_PROG + w, cls ? 2u : 1u);
#pragma unroll
        for (int i = 0; i < 8; ++i) {
            sa1 = MFMA32(kf[2 * i + 1], qf[i], i == 0 ? f32x16{} : sa1); SCHED0();
            if (i >= 2 && i < 6) { mx = i == 2 ? max2(sa0[0], sa0[1]) : max3(mx, sa0[4 * i - 8], sa0[4 * i - 7]); mx = max3(mx, sa0[4 * i - 6], sa0[4 * i - 5]); PIN(mx); }
            SCHED0();
        }
        lds_st(sm, M_PROG + w, 2u);
        wait_full(sm, 2u, fw);
        SET_K(1u);
#pragma unroll
        for (int i = 0; i < 7; ++i) KRD(i);
        SCHED0();
        { float m2 = max3(sa1[0], sa1[1], sa1[2]);
#pragma unroll
          for (int e = 3; e < 9; e += 2) { m2 = max3(m2, sa1[e], sa1[e + 1]); mx = max3(mx, sa1[e + 6], sa1[e + 7]); }
          mx = max3(mx, m2, sa1[15]);
          XHALF(mx);
          sa0 -= mx; sa1 -= mx;
#pragma unroll
          for (int i = 0; i < 16; ++i) negm[i] = -mx; }
        PIN(negm);
        SCHED0();
#define STEP(S0, S1, N0, N1) do { v2u fv_; if (STAMP > 1 && w == 1) TSTAMP(54 + (tk >> 1)); \
        _Pragma("unroll") for (int i = 0; i < 16; ++i) { \
            if (!(i & 1)) USE(kf[i + 1]); \
            if (i == 0) N0 = MFMA32(kf[0], qf[0], negm); else if (i == 1) N1 = MFMA32(kf[1], qf[0], negm); \
            else if (i & 1) N1 = MFMA32(kf[i], qf[i >> 1], N1); else N0 = MFMA32(kf[i], qf[i >> 1], N0); \
            SCHED0(); \
            if (i < 9) KRD(i + 7); \
            if (i == 4) fv_ = *(const volatile LAS v2u*)(sm + LDS_M + 4 * M_FULL); \
            if (i == 9) { const unsigned f_ = __builtin_amdgcn_readfirstlane(umin(fv_[0], fv_[1])); fw = fw > f_ ? fw : f_; NEED(tk + 1u); SET_V(tk + 1u); } \
            if (i >= 9) VRD(i - 9); \
            SCHED0(); \
            if (i < 8) { EXP1(S0, S1, i + (i >> 1)); if (i & 1) EXP1(S0, S1, i + (i >> 1) + 1); } else EXP1(S0, S1, i + 4); \
            SCHED0(); } \
        float mx_; \
        _Pragma("unroll") for (int i = 0; i < 16; ++i) { \
            if (!(i & 1)) USE(vhi[i + 1]); \
            PV(i); \
            SCHED0(); \
            if (i < 9) VRD(i + 7); \
            if (i == 9) { if (tk < 13u) NEED(tk + 2u); SET_K(tk + 2u); }     \
            if (i >= 9) KRD(i - 9); \
            SCHED0(); \
            if (i < 10) EXP1(S0, S1, 20 + i); \
            if (i == 10) { EXP1(S0, S1, 30); EXP1(S0, S1, 31); FIN1(31); } \
            if (i == 11) { mx_ = max2(N0[0], N0[1]); mx_ = max3(mx_, N0[2], N0[3]); mx_ = max3(mx_, N0[4], N0[5]); mx_ = max3(mx_, N0[6], N0[7]); PIN(mx_); } \
            if (i >= 12 && i < 15) { _Pragma("unroll") for (int e = 8 * (i - 11); e < 8 * (i - 10); e += 2) mx_ = max3(mx_, SV(N0, N1, e), SV(N0, N1, e + 1)); PIN(mx_); } \
            if (i == 13) { S0 = N0; PIN(S0); } \
            if (i == 15) { S1 = N1; PIN(S1); } \
            SCHED0(); } \
        tk += 2u; \
        lds_st(sm, M_PROG + w, tk + 1u); \
        if (__builtin_expect(__builtin_amdgcn_ballot_w64(mx_ > THR) != 0ull, 0)) {     \
            XHALF(mx_); \
            const float dl_ = max2(mx_, 0.f), al_ = __builtin_amdgcn_exp2f(-dl_); \
            lsum *= al_; ls2 *= al_; \
            _Pragma("unroll") for (int i = 0; i < 16; ++i) { S0[i] -= dl_; S1[i] -= dl_; negm[i] -= dl_; o[0][i] *= al_; o[1][i] *= al_; o[2][i] *= al_; o[3][i] *= al_; } } \
        SCHED0(); } while (0)
#pragma unroll 1
        while (tk < 15u) STEP(sa0, sa1, sb0, sb1);
        const unsigned pgv = *(const volatile LAS unsigned*)(sm + LDS_M + 4 * (M_PROG + (lane & 3)));
#pragma unroll
        for (int e = 0; e < 6; ++e) EXP1(sa0, sa1, e);
        if (w == 0) TSTAMP(13);
        wait_full(sm, 16u, fw);
        SET_V(15u);
        SCHED0();
#define TI(i) ((i) < 8 ? (i) : 8 + 4 * ((i) & 1) + (((i) - 8) >> 1))
#pragma unroll
        for (int i = 0; i < 7; ++i) VRD(TI(i));
#pragma unroll
        for (int e = 6; e < 16; ++e) EXP1(sa0, sa1, e);
        if (__builtin_amdgcn_ballot_w64(pgv < 10u) != 0ull) wait_prog(sm, 10u, lane);
        const int row = 32 * w + r;
        const unsigned oa = smb + LDS_X + row * 128 + 8 * hh + (((row >> 1) & 7) << 4);
        unsigned oad[8];
        v2u op[16];
        float inv;
#define OPK(i) do { op[i] = v2u{pk2s(o[(i) >> 2][4 * ((i) & 3)], o[(i) >> 2][4 * ((i) & 3) + 1], inv), pk2s(o[(i) >> 2][4 * ((i) & 3) + 2], o[(i) >> 2][4 * ((i) & 3) + 3], inv)}; PIN(op[i]); } while (0)
#define OST(i) (*(LAS v2u*)((lptr)(size_t)oad[(i) & 7] + ((i) >> 3) * XSLOT) = op[i])
        SCHED0();
#pragma unroll
        for (int i = 0; i < 16; ++i) {
            if (!(i & 1)) USE(vhi[TI(i + 1)]);
            PV(TI(i));
            SCHED0();
            if (i < 9) VRD(TI(i + 7));
            if (i >= 12) OST(i - 12);
            SCHED0();
            if (i < 8) { EXP1(sa0, sa1, 16 + 2 * i); EXP1(sa0, sa1, 17 + 2 * i); }
            if (i == 7) FIN1(31);
            if (i == 8 || i == 10) { _Pragma("unroll") for (int g = 2 * i - 16; g < 2 * i - 12; ++g) { oad[g] = oa ^ (g << 4); PIN(oad[g]); } }
            if (i == 9) { lsum += ls2; auto r2 = __builtin_amdgcn_permlane32_swap(__float_as_uint(lsum), __float_as_uint(lsum), false, false); inv = __builtin_amdgcn_rcpf(__uint_as_float(r2[0]) + __uint_as_float(r2[1])); PIN(inv); }
            if (i >= 11) OPK(i - 11);
            SCHED0();
        }
        lds_st(sm, M_PROG + w, 17u);
        if (w == 0) TSTAMP(3);
        if (w == 2) TSTAMP(14);
        OPK(5); OPK(6); OPK(7); OPK(8); SCHED0(); OST(4); OST(5); OST(6); OST(7); SCHED0();
        OPK(9); OPK(10); OPK(11); OPK(12); SCHED0(); OST(8); OST(9); OST(10); OST(11); SCHED0();
        OPK(13); OPK(14); OPK(15); SCHED0(); OST(12); OST(13); OST(14); OST(15);
    }
    {
        const unsigned woff = smb + LDS_W + (8 * rw + q4) * 256 + (((8 * (hg >> 1) + 2 * w + (p >> 1)) ^ (2 * q4 + 8 * (rw & 1))) << 4) + 8 * (p & 1), wx = 128u;
        const unsigned xb = smb + LDS_X + ch * 128 + ((rw ^ (ch >> 1)) << 4), xlo = xb + (hg & 1) * 3 * 2048, xhi = xb + ((hg & 1) ^ 1) * 3 * 2048;
        f32x4 acc[12];
        GFrag fa, fb;
        unsigned wsl = 3, xsl = 0, wa0, wa1, xa0, xa1, xa2, xa3;
        GADDR();
        WAIT_LGKM(0);
        BAR();
        {
            const rsrc_t sO = __builtin_amdgcn_make_buffer_rsrc((void*)(ws + WS_O + (size_t)bh * 24576), 0, 24576, 0x00020000);
            v4u t[6];
#pragma unroll
            for (int i = 0; i < 6; ++i) t[i] = *(const LAS v4u*)(sm + LDS_X + w * 6144 + i * 1024 + lane * 16);
#pragma unroll
            for (int i = 0; i < 6; ++i) __builtin_amdgcn_raw_buffer_store_b128(t[i], sO, lane * 16 + (i & 3) * 1024, w * 6144 + (i >> 2) * 4096, 16);
        }
        if (w == 0) TSTAMP(16);
        const unsigned okw = *(const volatile LAS unsigned*)sm;
        GSTEP(7, false, fa, fb, false, false);
        GSTEP(7, false, fb, fa, true, true);
        bool pend = true;
        const bool lazy = cls && __builtin_amdgcn_readfirstlane(okw) != 0;
#define TRY_FLAG(force) do { if (pend) { unsigned st_; asm volatile("s_getreg_b32 %0, hwreg(HW_REG_IB_STS, 0, 4)" : "=s"(st_) :: "memory"); \
            if ((force) || st_ == 0u) { WAIT_VM(0); pend = false; if (lane == 0) put_flag((GAS u64*)(ws + WS_FLAG1 + (size_t)(b * 4 + hg) * 256) + hl * 4 + w, epoch); if (w == 0) TSTAMP(12); } } } while (0)
#pragma unroll 1
        for (int i = 2; i < 16; i += 2) { TRY_FLAG(!lazy || i >= 4); GSTEP(7, true, fa, fb, true, false); TRY_FLAG(false); GSTEP(7, true, fb, fa, true, false); }
        asm volatile("s_nop 7");
        const rsrc_t rP = __builtin_amdgcn_make_buffer_rsrc((void*)(ws + WS_PART + ((size_t)(b * 32 + hl) * 4 + w) * 12288), 0, 25 * 4 * 12288, 0x00020000);
        GAS u64* f2 = (GAS u64*)(ws + WS_FLAG2 + ((size_t)(b * 32 + hl) * 4 + w) * 64);
#define AT(j, k) acc[2 * (3 * ((j) & 1) + (k)) + ((j) >> 1)]
#define PST(j) do { const unsigned so_ = (hg ^ (j)) * (8 * 4 * 12288) + hg * 3072; \
            __builtin_amdgcn_raw_buffer_store_b128(v4u{pk2(AT(j, 0)[0], AT(j, 0)[1]), pk2(AT(j, 0)[2], AT(j, 0)[3]), pk2(AT(j, 1)[0], AT(j, 1)[1]), pk2(AT(j, 1)[2], AT(j, 1)[3])}, rP, lane * 16, so_, 16); \
            __builtin_amdgcn_raw_buffer_store_b64(v2u{pk2(AT(j, 2)[0], AT(j, 2)[1]), pk2(AT(j, 2)[2], AT(j, 2)[3])}, rP, lane * 8 + 1024, so_, 16); } while (0)
        SCHED0();
#pragma unroll
        for (int e = 0; e < 12; ++e) {
            const int j = e < 3 ? 2 : e < 6 ? 1 : e < 9 ? 3 : 0, n = 2 * (3 * (j & 1) + e % 3) + (j >> 1);
            GM(fb, 2 * n, false); GM(fb, 2 * n + 1, false); SCHED0();
            if (e == 4) PST(2);
            if (e == 7) PST(1);
            if (e == 10) PST(3);
            SCHED0();
        }
        if (w == 0) TSTAMP(4);
        WAIT_VM(0);
        if (lane < 4 && lane != hg) put_flag(f2 + lane * (8 * 4 * 8) + hg, epoch);
        if (w == 0) TSTAMP(15);
        wait_flags(f2 + hg * (8 * 4 * 8) + ((lane & 3) == hg ? ((hg + 1) & 3) : (lane & 3)), epoch);
        if (w == 0) TSTAMP(5);
        v4u pt[3]; v2u pc[3];
#pragma unroll
        for (int i = 0; i < 3; ++i) {
            const unsigned so = hg * (8 * 4 * 12288) + (hg ^ (i + 1)) * 3072;
            pt[i] = __builtin_amdgcn_raw_buffer_load_b128(rP, lane * 16, so, 16);
            pc[i] = __builtin_amdgcn_raw_buffer_load_b64(rP, lane * 8 + 1024, so, 16);
        }
        char* orow = outg + (size_t)(b * 96 + 48 * (hg & 1) + ch) * 2048 + (nt * 128 + 64 * (hg >> 1) + 16 * w + 4 * rw) * 2;
#pragma unroll
        for (int k = 0; k < 3; ++k) {
            f32x4 v = AT(0, k);
#pragma unroll
            for (int i = 0; i < 3; ++i) { const unsigned u0 = k < 2 ? pt[i][2 * k] : pc[i][0], u1 = k < 2 ? pt[i][2 * k + 1] : pc[i][1];
                v += f32x4{__uint_as_float(u0 << 16), __uint_as_float(u0 & 0xffff0000u), __uint_as_float(u1 << 16), __uint_as_float(u1 & 0xffff0000u)}; }
            *(GAS v2u*)(orow + (size_t)k * 16 * 2048) = v2u{pk2(v[0], v[1]), pk2(v[2], v[3])};
        }
        if (hg == 0 && hl == 0 && w == 0 && lane == 0) __hip_atomic_store((GAS u64*)(ws + WS_EPOCH + b * 256), epoch, RLX_AGENT);
    }
}

extern "C" void kernel_launch(void* const* d_in, const int* in_sizes, int n_in, void* d_out, int out_size, void* d_ws, size_t ws_size, hipStream_t stream) {
    mha_fused<<<256, 512, 0, stream>>>((const char*)d_in[0], (const char*)d_in[1], (const char*)d_in[2], (const char*)d_in[3], (const char*)d_in[4], (char*)d_out, (char*)d_ws);
}
```

```cpp
#include <hip/hip_runtime.h>
#include <hip/hip_bf16.h>

typedef unsigned v4u __attribute__((ext_vector_type(4)));
typedef unsigned v2u __attribute__((ext_vector_type(2)));
typedef short bf16x8 __attribute__((ext_vector_type(8)));
typedef short s16x4 __attribute__((ext_vector_type(4)));
typedef float f32x16 __attribute__((ext_vector_type(16)));
typedef float f32x4 __attribute__((ext_vector_type(4)));
typedef float f32x2 __attribute__((ext_vector_type(2)));
typedef __bf16 bf16x2 __attribute__((ext_vector_type(2)));
typedef unsigned long long u64;
typedef __amdgpu_buffer_rsrc_t rsrc_t;
#define LAS __attribute__((address_space(3)))
#define GAS __attribute__((address_space(1)))
#define DI __device__ __forceinline__
typedef LAS char* lptr;

#define POL_KV " nt"
constexpr float THR = 8.f;
constexpr int POS_B = 3;

constexpr int WSLOT = 16384, XSLOT = 12288;
constexpr int LDS_W = 0, LDS_X = 7 * WSLOT, LDS_BYTES = LDS_X + 4 * XSLOT;
constexpr int LDS_M = LDS_BYTES - 256;
constexpr u64 SLOT_TAB = 0x2106543872106543ull;
constexpr int M_FULL = 0, M_EPOCH = 4, M_PROG = 8;
constexpr size_t WS_EPOCH = 0;
constexpr size_t WS_FLAG1 = 4096;
constexpr size_t WS_FLAG2 = WS_FLAG1 + 32 * 256;
constexpr size_t WS_O = 131072;
constexpr size_t WS_PART = WS_O + 256 * 24576;
constexpr unsigned SPIN_CAP = 1u << 18;

DI unsigned pk2(float a, float b) { f32x2 v = {a, b}; return __builtin_bit_cast(unsigned, __builtin_convertvector(v, bf16x2)); }
DI unsigned pk2s(float a, float b, float s) { f32x2 v = {a, b}; v *= s; return __builtin_bit_cast(unsigned, __builtin_convertvector(v, bf16x2)); }
DI float max2(float a, float b) { return __builtin_elementwise_maximum(a, b); }
DI float max3(float a, float b, float c) { return max2(max2(a, b), c); }
DI unsigned umin(unsigned a, unsigned b) { return a < b ? a : b; }
DI unsigned lds_ld(lptr sm, int word) {
    unsigned r; const unsigned a = (unsigned)(size_t)(sm + LDS_M) + 4u * (unsigned)word;
    asm volatile("ds_read_b32 %0, %1\n\ts_waitcnt lgkmcnt(0)" : "=v"(r) : "v"(a) : "memory"); return r; }
DI v2u lds_ld2(lptr sm, int word) {
    v2u r; const unsigned a = (unsigned)(size_t)(sm + LDS_M) + 4u * (unsigned)word;
    asm volatile("ds_read_b64 %0, %1\n\ts_waitcnt lgkmcnt(0)" : "=v"(r) : "v"(a) : "memory"); return r; }
DI unsigned min4(v4u r) { return umin(umin(r[0], r[1]), umin(r[2], r[3])); }
DI unsigned lds_full(lptr sm) {
    v4u r; const unsigned a = (unsigned)(size_t)(sm + LDS_M) + 4u * M_FULL;
    asm volatile("ds_read_b128 %0, %1\n\ts_waitcnt lgkmcnt(0)" : "=v"(r) : "v"(a) : "memory"); return __builtin_amdgcn_readfirstlane(min4(r)); }
DI void lds_st(lptr sm, int word, unsigned v) {
    const unsigned a = (unsigned)(size_t)(sm + LDS_M) + 4u * (unsigned)word;
    asm volatile("ds_write_b32 %0, %1" :: "v"(a), "v"(v) : "memory"); }
DI void lds_max(lptr sm, int word, unsigned v, int lane) {
    const unsigned a = (unsigned)(size_t)(sm + LDS_M) + 4u * (unsigned)word;
    if (lane == 0) asm volatile("ds_max_u32 %0, %1" :: "v"(a), "v"(v) : "memory"); }
#define CFENCE() asm volatile("" ::: "memory")
#define SCHED0() __builtin_amdgcn_sched_barrier(0)
#define WAIT_LGKM(n) do { __builtin_amdgcn_s_waitcnt(0xC07F | ((n) << 8)); asm volatile("" ::: "memory"); } while (0)
#define WAIT_VM(n) asm volatile("s_waitcnt vmcnt(%0)" :: "i"(n) : "memory")
#define BAR() asm volatile("s_barrier" ::: "memory")
#define PIN(x) asm volatile("" : "+v"(x))
#define MFMA32(a, b, c) __builtin_amdgcn_mfma_f32_32x32x16_bf16(a, b, c, 0, 0, 0)
#define RLX_AGENT __ATOMIC_RELAXED, __HIP_MEMORY_SCOPE_AGENT
DI void put_flag(GAS u64* p, u64 v) { (void)__hip_atomic_exchange(p, v, RLX_AGENT); }

DI void wait_full(lptr sm, unsigned need, unsigned& cur) {
    unsigned spins = 0;
#pragma nounroll
    while (__builtin_expect(cur < need, 0)) {
        cur = lds_full(sm);
        if (cur >= need || ++spins > SPIN_CAP) break;
        __builtin_amdgcn_s_sleep(1);
    }
    CFENCE();
}
DI void wait_prog(lptr sm, unsigned need, int lane) {
    unsigned spins = 0;
#pragma nounroll
    while (__builtin_amdgcn_ballot_w64(lds_ld(sm, M_PROG + (lane & 3)) < need) != 0ull && ++spins <= SPIN_CAP) __builtin_amdgcn_s_sleep(12);
    CFENCE();
}
DI void wait_flags(GAS u64* fl, u64 epoch) {
    unsigned spins = 0;
#pragma nounroll
    for (;;) {
        const u64 f = __hip_atomic_load(fl, RLX_AGENT);
        if (__builtin_amdgcn_ballot_w64(f != epoch) == 0ull || ++spins > SPIN_CAP) break;
        __builtin_amdgcn_s_sleep(1);
    }
    CFENCE();
}

DI v4u mk_srd(const void* p, unsigned bytes) { const u64 a = (u64)p; return v4u{(unsigned)a, (unsigned)(a >> 32) & 0xffffu, bytes, 0x00020000u}; }
#define DMA4(POL, srd, dst, v, soff) asm volatile("s_mov_b32 m0, %0\n\ts_nop 0\n\tbuffer_load_dwordx4 %3, %1, %2 offen" POL " lds\n\tbuffer_load_dwordx4 %4, %1, %2 offen offset:1024" POL " lds\n\t" \
        "buffer_load_dwordx4 %5, %1, %2 offen offset:2048" POL " lds\n\tbuffer_load_dwordx4 %6, %1, %2 offen offset:3072" POL " lds" \
        :: "s"((unsigned)(dst)), "s"(srd), "s"((unsigned)(soff)), "v"(v[0]), "v"(v[1]), "v"(v[2]), "v"(v[3]) : "memory")
#define DMA3(POL, srd, dst, v, soff) asm volatile("s_mov_b32 m0, %0\n\ts_nop 0\n\tbuffer_load_dwordx4 %3, %1, %2 offen" POL " lds\n\tbuffer_load_dwordx4 %4, %1, %2 offen offset:1024" POL " lds\n\t" \
        "buffer_load_dwordx4 %5, %1, %2 offen offset:2048" POL " lds" \
        :: "s"((unsigned)(dst)), "s"(srd), "s"((unsigned)(soff)), "v"(v[0]), "v"(v[1]), "v"(v[2]) : "memory")

struct GFrag { bf16x8 xa[12]; s16x4 wl[4], wh[4]; };
#define GM(F, m, Z) (acc[(m) >> 1] = __builtin_amdgcn_mfma_f32_16x16x32_bf16(__builtin_shufflevector(F.wl[(m) & 3], F.wh[(m) & 3], 0, 1, 2, 3, 4, 5, 6, 7), F.xa[2 * ((m) >> 2) + ((m) & 1)], \
                                                                             (Z) && !((m) & 1) ? f32x4{} : acc[(m) >> 1], 0, 0, 0))
#define GMA(F, m, Z) do { const bf16x8 a_ = __builtin_shufflevector(F.wl[(m) & 3], F.wh[(m) & 3], 0, 1, 2, 3, 4, 5, 6, 7); \
        if ((Z) && !((m) & 1)) asm volatile("v_mfma_f32_16x16x32_bf16 %0, %1, %2, 0" : "=&v"(acc[(m) >> 1]) : "v"(a_), "v"(F.xa[2 * ((m) >> 2) + ((m) & 1)])); \
        else asm volatile("v_mfma_f32_16x16x32_bf16 %0, %1, %2, %0" : "+v"(acc[(m) >> 1]) : "v"(a_), "v"(F.xa[2 * ((m) >> 2) + ((m) & 1)])); } while (0)
#define GADDR() do { wa0 = wsl * WSLOT + woff; wa1 = wa0 ^ wx; xa0 = xsl * XSLOT + xlo; xa1 = xa0 ^ 64u; xa2 = xsl * XSLOT + xhi; xa3 = xa2 ^ 64u; PIN(wa0); PIN(wa1); PIN(xa0); PIN(xa1); PIN(xa2); PIN(xa3); } while (0)
#define GSTEP(RING, DOBAR, FCUR, FPREV, HASPREV, Z) do { \
        if (DOBAR) BAR(); \
        SCHED0(); \
        _Pragma("unroll") for (int m = 0; m < 24; ++m) { \
            if (HASPREV) GMA(FPREV, m, Z); \
            SCHED0(); \
            if (m < 8) { const lptr wt = (lptr)(size_t)((m & 4) ? wa1 : wa0) + ((m >> 1) & 1) * 32 * 256 + (m & 1) * 4 * 256; \
                if (m & 1) FCUR.wh[m >> 1] = __builtin_amdgcn_ds_read_tr16_b64_v4i16((LAS s16x4*)wt); else FCUR.wl[m >> 1] = __builtin_amdgcn_ds_read_tr16_b64_v4i16((LAS s16x4*)wt); } \
            else if (m < 20) FCUR.xa[m - 8] = *(const LAS bf16x8*)((lptr)(size_t)(m < 14 ? ((m & 1) ? xa1 : xa0) : ((m & 1) ? xa3 : xa2)) + (((m - 8) >> 1) % 3) * 2048); \
            else if (m == 20) { wsl = (wsl + 1 == (RING)) ? 0 : wsl + 1; xsl = (xsl + 1) & 3; wa0 = wsl * WSLOT + woff; wa1 = wa0 ^ wx; PIN(wa0); PIN(wa1); } \
            else if (m == 21) { xa0 = xsl * XSLOT + xlo; xa1 = xa0 ^ 64u; PIN(xa0); PIN(xa1); } \
            else if (m == 22) { xa2 = xsl * XSLOT + xhi; xa3 = xa2 ^ 64u; PIN(xa2); PIN(xa3); } \
            SCHED0(); } \
        WAIT_LGKM(12); } while (0)

__global__ __launch_bounds__(512, 2) void mha_fused(const char* __restrict__ xg, const char* __restrict__ kg, const char* __restrict__ vg,
                                                    const char* __restrict__ wqg, const char* __restrict__ wog, char* __restrict__ outg, char* __restrict__ ws) {
    __shared__ __attribute__((aligned(16))) char smem[LDS_BYTES];
    const lptr sm = (lptr)smem;
    const unsigned smb = (unsigned)(size_t)sm;
    const int tid = threadIdx.x, lane = tid & 63, w = __builtin_amdgcn_readfirstlane(tid >> 6);
    const int wg = blockIdx.x, xcd = wg & 7, idx = wg >> 3;
    const int hg = xcd >> 1, b = idx >> 2, hl = 4 * (xcd & 1) + (idx & 3), h = 8 * hg + hl;
    const int nt = (hl & 4) + ((hl + b) & 3);
    const int bh = b * 32 + h;
    const int cls = hl & 1;
    const int rw = lane >> 4, ch = lane & 15, lw = w & 3;
    const int r = lane & 31, hh = lane >> 5, blk = (lane >> 4) & 1, q4 = (lane & 15) >> 2, p = lane & 3;
    u64 epoch = 0;
    u64 code0; unsigned code_len;
    asm volatile("s_getpc_b64 %0\n.Lmha_code_begin:\n\ts_mov_b32 %1, .Lmha_code_end-.Lmha_code_begin" : "=s"(code0), "=s"(code_len));

    if (__builtin_expect(w >= 4, 1)) {
        const int rot = (4 * hl + 2 * b) & 15;
        const v4u rq = mk_srd(wqg + h * 256 + lw * 16 * 8192, 1024 * 8192 - (h * 256 + lw * 16 * 8192));
        const v4u rx = mk_srd(xg + (size_t)(b * 96 + lw * 24) * 2048, 24 * 2048);
        unsigned vq[4], vx[3];
#pragma unroll
        for (int i = 0; i < 4; ++i) vq[i] = (4 * i + rw) * 8192 - i * 1024 + ((ch ^ (2 * rw + 8 * (i >> 1))) << 4);
#pragma unroll
        for (int i = 0; i < 3; ++i) vx[i] = (8 * i + (lane >> 3)) * 2048 - i * 1024 + (((lane & 7) ^ (4 * ((3 * lw + i) & 1) + rw)) << 4);
        const unsigned wdst = smb + LDS_W + lw * 4096, xdst = smb + LDS_X + lw * 3072;
#define P1_ISSUE(i) do { const unsigned kt_ = (unsigned)((i) + rot) & 15u; \
            DMA3("", rx, xdst + ((i) & 3) * XSLOT, vx, kt_ * 128u); \
            DMA4("", rq, wdst + (((i) + 2) % 3) * WSLOT, vq, kt_ * (64u * 8192u)); } while (0)
        P1_ISSUE(0);
        SCHED0();
        __builtin_amdgcn_s_setprio(2);
        P1_ISSUE(1);
        const bool early = w >= 6 && cls == 1, parker = w < 6 && cls == 1;
        const v4u rk = mk_srd(kg + (size_t)bh * 131072 - 1024, 131072 + 1024), rv = mk_srd(vg + (size_t)bh * 131072 - 1024, 131072 + 1024);
        unsigned vk[4];
#pragma unroll
        for (int i = 0; i < 4; ++i) vk[i] = (8 * (i >> 1) + ((lane >> 2) & 7)) * 256 + (i & 1) * 128 + (lane >> 5) * 64 + (((lane & 3) ^ ((2 * (i >> 1) + ((lane >> 4) & 1)) & 3)) << 4) - i * 1024 + 1024;
        const unsigned hw = (w & 1) * 8192;
#define K_ISSUE(slot, j) do { DMA4(POL_KV, rk, smb + LDS_W + (slot) * WSLOT + hw, vk, (j) * 16384 + hw); DMA4(POL_KV, rk, smb + LDS_W + (slot) * WSLOT + hw + 4096, vk, (j) * 16384 + hw + 4096); } while (0)
#define V_ISSUE(slot, j) do { DMA4(POL_KV, rv, smb + LDS_W + (slot) * WSLOT + hw, vk, (j) * 16384 + hw); DMA4(POL_KV, rv, smb + LDS_W + (slot) * WSLOT + hw + 4096, vk, (j) * 16384 + hw + 4096); } while (0)
        v4u park[3][8];
#define PARK_LD4(d, o, srd, v, soff) asm volatile("buffer_load_dwordx4 %0, %6, %4, %5 offen" POL_KV "\n\tbuffer_load_dwordx4 %1, %7, %4, %5 offen offset:1024" POL_KV "\n\t" \
            "buffer_load_dwordx4 %2, %8, %4, %5 offen offset:2048" POL_KV "\n\tbuffer_load_dwordx4 %3, %9, %4, %5 offen offset:3072" POL_KV \
            : "=&v"(d[o]), "=&v"(d[o + 1]), "=&v"(d[o + 2]), "=&v"(d[o + 3]) : "s"(srd), "s"((unsigned)(soff)), "v"(v[0]), "v"(v[1]), "v"(v[2]), "v"(v[3]) : "memory")
#define PARK_LD(n, srd, v, j) do { PARK_LD4(park[n], 0, srd, v, (j) * 16384 + hw); PARK_LD4(park[n], 4, srd, v, (j) * 16384 + hw + 4096); } while (0)
#define PARK_WR(n) do { _Pragma("unroll") for (int i_ = 0; i_ < 8; ++i_) *(LAS v4u*)(sm + LDS_W + (n) * WSLOT + hw + i_ * 1024 + lane * 16) = park[n][i_]; } while (0)
        WAIT_VM(7); BAR();
        P1_ISSUE(2);
#pragma unroll
        for (int i = 1; i < 14; ++i) {
            if ((i == POS_B + 1 || i == POS_B + 2) && early) WAIT_VM(39); else if ((i == POS_B + 1 || i == POS_B + 2) && parker) WAIT_VM(31); else WAIT_VM(7);
            BAR();
            P1_ISSUE(i + 2);
            if (i == POS_B && early) { K_ISSUE(3, 0); K_ISSUE(4, 1); V_ISSUE(5, 0); K_ISSUE(6, 2); }
            if (i == POS_B && parker) { PARK_LD(0, rv, vk, 1); PARK_LD(1, rk, vk, 3); PARK_LD(2, rv, vk, 2); }
        }
        WAIT_VM(7);
        BAR();
        if (w < 6 || early) WAIT_VM(0); else { K_ISSUE(3, 0); WAIT_VM(8); }
        BAR();
        if (w >= 6 && !early) K_ISSUE(4, 1);
        BAR();
        if (w >= 6 && !early) { V_ISSUE(5, 0); K_ISSUE(6, 2); }
        if (w == 4 && lane < 16 && lane != M_EPOCH && lane != M_EPOCH + 1) lds_st(sm, lane, lane == M_PROG + 3 ? 1000u : (lane >> 1) == 1 ? (parker ? 4u : 1000u) : 0u);
        SCHED0();
        const unsigned ord = cls ? 0x62473510u : 0x73516240u;
#define KT(i) ((((unsigned)hl + (ord >> (4u * ((unsigned)(i) >> 1)))) & 7u) * 2u + ((unsigned)(i) & 1u))
        const v4u ro = mk_srd(wog + (size_t)hg * 1024 * 2048 + nt * 256, 1024 * 2048 - nt * 256);
        unsigned vo[4];
#pragma unroll
        for (int i = 0; i < 4; ++i) vo[i] = (4 * i + rw) * 2048 - i * 1024 + ((ch ^ (2 * rw + 8 * (i >> 1))) << 4);
        const v4u rO = mk_srd(ws + WS_O + (size_t)(b * 32 + hg * 8) * 24576, 8 * 24576);
        GAS u64* f1 = (GAS u64*)(ws + WS_FLAG1 + (size_t)(b * 4 + hg) * 256);
        GAS u64* f1a = f1 + (lane & 31);
        unsigned rdy = 0;
#define HEAD_OK(n) (((rdy >> (2u * KT(2 * (n)))) & 7u) == 7u)
#define HEAD_WAIT(n) do { unsigned sp_ = 0; _Pragma("nounroll") while (!HEAD_OK(n) && ++sp_ <= SPIN_CAP) { if (sp_ > 1) __builtin_amdgcn_s_sleep(4); rdy = (unsigned)__builtin_amdgcn_ballot_w64(__hip_atomic_load(f1a, RLX_AGENT) == epoch); } CFENCE(); } while (0)
        if (w < 6) {
            WAIT_LGKM(0);
            BAR();
            if (parker) { __builtin_amdgcn_s_setprio(0); PARK_WR(0); PARK_WR(1); lds_st(sm, M_FULL + 2 + (w & 1), 6u); PARK_WR(2); lds_st(sm, M_FULL + 2 + (w & 1), 1000u); __builtin_amdgcn_s_setprio(2); }
            if (w == 4) {
                const v2u e = lds_ld2(sm, M_EPOCH); epoch = ((u64)__builtin_amdgcn_readfirstlane(e[1]) << 32) | (unsigned)__builtin_amdgcn_readfirstlane(e[0]);
                unsigned v0[4];
#pragma unroll
                for (int i = 0; i < 4; ++i) v0[i] = lane * 16;
#define O_WHOLE(i) do { _Pragma("unroll") for (int g_ = 0; g_ < 3; ++g_) DMA4(" sc1", rO, smb + LDS_X + (i) * XSLOT + g_ * 4096, v0, KT(i) * (unsigned)XSLOT + g_ * 4096); } while (0)
                bool ok = false; u64 fv1 = ~epoch; PIN(fv1);
                if (cls) {
                    unsigned spins = 0, pv;
#pragma nounroll
                    while (__builtin_amdgcn_ballot_w64(lds_ld(sm, M_PROG + (lane & 3)) < 16u) != 0ull && ++spins <= SPIN_CAP) __builtin_amdgcn_s_sleep(16);
                    asm volatile("global_load_dwordx2 %0, %1, off sc1" : "+v"(fv1) : "v"(f1a) : "memory");
#pragma nounroll
                    for (;;) {
                        unsigned st; asm volatile("s_getreg_b32 %0, hwreg(HW_REG_IB_STS, 0, 4)" : "=s"(st) :: "memory");
                        if (st == 0u) {
                            asm volatile("s_waitcnt vmcnt(0)" : "+v"(fv1) :: "memory");
                            rdy = (unsigned)__builtin_amdgcn_ballot_w64(fv1 == epoch); ok = HEAD_OK(1);
                            if (ok) break;
                            __builtin_amdgcn_s_sleep(4);
                            asm volatile("global_load_dwordx2 %0, %1, off sc1" : "+v"(fv1) : "v"(f1a) : "memory");
                        }
                        __builtin_amdgcn_s_sleep(3);
                        pv = lds_ld(sm, M_PROG + (lane & 3));
                        if (__builtin_amdgcn_ballot_w64(pv == 17u) != 0ull || ++spins > SPIN_CAP) break;
                    }
                    if (ok) O_WHOLE(2);
                    *(volatile LAS unsigned*)sm = (ok && HEAD_OK(2)) ? 1u : 0u;
                    WAIT_LGKM(0);
                }
                BAR();
                if (!ok) {
                    if (!cls) __builtin_amdgcn_s_sleep(40);
                    asm volatile("s_waitcnt vmcnt(0)" : "+v"(fv1) :: "memory");
                    rdy = (unsigned)__builtin_amdgcn_ballot_w64(fv1 == epoch); HEAD_WAIT(1);
                    O_WHOLE(2);
                }
                O_WHOLE(3);
                HEAD_WAIT(2);
                WAIT_VM(12);
            }
        } else {
            WAIT_VM(24);
            BAR();
            if (!early) { WAIT_VM(16); lds_st(sm, M_FULL + (w & 1), 2u); }
            const unsigned pa_ = smb + LDS_M + 4u * (M_PROG + (lane & 3)), fa_ = smb + LDS_M + 4u * (M_FULL + (w & 1));
            {
                const unsigned t0 = early ? 7u : 4u;
                const u64 kb_ = (u64)(kg + (size_t)bh * 131072 + 16384 - 1024 + hw), vb_ = (u64)(vg + (size_t)bh * 131072 - 1024 + hw);
                unsigned pv, tv;
                asm volatile(
                    "s_mov_b32 s88, %[t0]\n\ts_mov_b64 s[90:91], %[tab]\n\ts_mov_b32 s94, %[nrec]\n\ts_mov_b32 s95, 0x20000\n\ts_mov_b32 s97, %[cap]\n"
                    "1:\n\ts_waitcnt vmcnt(15)\n\ts_sub_i32 s89, s88, 7\n\ts_cmp_eq_u32 s88, 7\n\ts_addc_u32 s89, s89, 0\n"
                    "2:\n\ts_getreg_b32 s96, hwreg(HW_REG_IB_STS, 0, 4)\n\tds_read_b32 %[pv], %[pa]\n\ts_add_u32 s96, s96, 7\n\ts_lshr_b32 s96, s96, 3\n\ts_sub_u32 s96, s88, s96\n\t"
                    "v_mov_b32 %[tv], s96\n\tds_write_b32 %[fa], %[tv]\n\ts_waitcnt lgkmcnt(1)\n\tv_cmp_gt_i32 vcc, s89, %[pv]\n\ts_cbranch_vccz 3f\n\t"
                    "s_sleep 4\n\ts_sub_u32 s97, s97, 1\n\ts_cbranch_scc0 2b\n\ts_mov_b32 s97, 0\n"
                    "3:\n\ts_and_b32 s96, s90, 15\n\ts_lshl_b32 s96, s96, 14\n\ts_add_u32 m0, s96, %[lb]\n\ts_bitcmp1_b32 %[km], s88\n\ts_cselect_b64 s[92:93], %[kb], %[vb]\n\t"
                    "s_sub_u32 s89, s88, 1\n\ts_lshl_b32 s89, s89, 13\n\ts_bitset0_b32 s89, 13\n\t"
                    "buffer_load_dwordx4 %[v0], s[92:95], s89 offen" POL_KV " lds\n\tbuffer_load_dwordx4 %[v1], s[92:95], s89 offen offset:1024" POL_KV " lds\n\t"
                    "buffer_load_dwordx4 %[v2], s[92:95], s89 offen offset:2048" POL_KV " lds\n\tbuffer_load_dwordx4 %[v3], s[92:95], s89 offen offset:3072" POL_KV " lds\n\t"
                    "s_add_u32 m0, m0, 0x1000\n\ts_bitset1_b32 s89, 12\n\t"
                    "buffer_load_dwordx4 %[v0], s[92:95], s89 offen" POL_KV " lds\n\tbuffer_load_dwordx4 %[v1], s[92:95], s89 offen offset:1024" POL_KV " lds\n\t"
                    "buffer_load_dwordx4 %[v2], s[92:95], s89 offen offset:2048" POL_KV " lds\n\tbuffer_load_dwordx4 %[v3], s[92:95], s89 offen offset:3072" POL_KV " lds\n\t"
                    "s_lshr_b64 s[90:91], s[90:91], 4\n\ts_add_u32 s88, s88, 1\n\ts_cmp_lt_u32 s88, 16\n\ts_cbranch_scc1 1b"
                    : [pv] "=&v"(pv), [tv] "=&v"(tv)
                    : [t0] "s"(t0), [tab] "s"(SLOT_TAB >> (4u * t0)), [nrec] "s"(131072u + 1024u - hw), [cap] "s"(SPIN_CAP), [pa] "v"(pa_), [fa] "v"(fa_), [lb] "s"(smb + hw), [km] "s"(0x2AAAu),
                      [kb] "s"(kb_), [vb] "s"(vb_), [v0] "v"(vk[0]), [v1] "v"(vk[1]), [v2] "v"(vk[2]), [v3] "v"(vk[3])
                    : "s88", "s89", "s90", "s91", "s92", "s93", "s94", "s95", "s96", "s97", "vcc", "scc", "memory");
            }
            const int fwd = M_FULL + (w & 1);
            WAIT_VM(16); lds_max(sm, fwd, 14u, lane);
            WAIT_VM(8); lds_st(sm, fwd, 15u);
            WAIT_VM(0); lds_st(sm, fwd, 16u);
#define WO_ISSUE2(i) do { const unsigned d_ = smb + LDS_W + (((i) + 3) % 7) * WSLOT + hw, so_ = (KT(i) * 64u + (w & 1) * 32u) * 2048u; \
            DMA4("", ro, d_, vo, so_); DMA4("", ro, d_ + 4096u, vo, so_ + 16u * 2048u); } while (0)
            wait_prog(sm, 12u, lane); WO_ISSUE2(0); WO_ISSUE2(1);
            wait_prog(sm, 14u, lane); WAIT_VM(8); WO_ISSUE2(2); WAIT_VM(8); WO_ISSUE2(3);
            WAIT_VM(0);
        }
        bool lazy = false;
#define O_FLAG() do { if (lane == 0) put_flag(f1 + hl * 4 + (w - 5), epoch); } while (0)
        if (w > 4) {
            const v2u e = lds_ld2(sm, M_EPOCH); epoch = ((u64)__builtin_amdgcn_readfirstlane(e[1]) << 32) | (unsigned)__builtin_amdgcn_readfirstlane(e[0]);
            const rsrc_t sO = __builtin_amdgcn_make_buffer_rsrc((void*)(ws + WS_O + (size_t)bh * 24576 + (w - 5) * 8192), 0, 8192, 0x00020000);
            unsigned ia = smb + LDS_X + (w - 5) * 8192 + lane * 16; PIN(ia);
            BAR();
            v4u t[8];
#pragma unroll
            for (int i = 0; i < 8; ++i) t[i] = *(const LAS v4u*)((lptr)(size_t)ia + i * 1024);
            const unsigned okw = *(const volatile LAS unsigned*)sm;
#pragma unroll
            for (int i = 0; i < 8; ++i) __builtin_amdgcn_raw_buffer_store_b128(t[i], sO, lane * 16 + (i & 3) * 1024, (i >> 2) * 4096, 16);
            lazy = cls && __builtin_amdgcn_readfirstlane(okw) != 0;
            if (!lazy) { WAIT_VM(0); O_FLAG(); }
        }
        unsigned vl[3];
#pragma unroll
        for (int i = 0; i < 3; ++i) vl[i] = lane * 16 + lw * 3072;
#define O_ISSUE(i) DMA3(" sc1", rO, xdst + ((i) & 3) * XSLOT, vl, KT(i) * (unsigned)XSLOT)
#define WO_ISSUE(i) DMA4("", ro, wdst + (((i) + 3) % 7) * WSLOT, vo, (KT(i) * 64u + lw * 16u) * 2048u)
        u64 fv = ~epoch; PIN(fv);
#define REFRESH(n) do { if (w == 4 && !HEAD_OK(n)) asm volatile("global_load_dwordx2 %0, %1, off sc1" : "+v"(fv) : "v"(f1a) : "memory"); } while (0)
        BAR();
        if (lazy) { WAIT_VM(0); O_FLAG(); }
        O_ISSUE(4); WO_ISSUE(4); REFRESH(3); WO_ISSUE(5);
        if (w == 4) WAIT_VM(11);
        BAR();
#pragma unroll
        for (int i = 3; i < 13; ++i) {
            if (!(i & 1) && i < 12) REFRESH((i + 4) >> 1);
            O_ISSUE(i + 2); WO_ISSUE(i + 3); WAIT_VM(11);
            if ((i & 1) && w == 4) { PIN(fv); rdy |= (unsigned)__builtin_amdgcn_ballot_w64(fv == epoch); HEAD_WAIT((i + 3) >> 1); }
            BAR();
        }
        O_ISSUE(15); WAIT_VM(4 + 3); BAR();
        WAIT_VM(0); BAR();
        return;
    }
    const unsigned kbase = 2048 * (r >> 3) + 64 * (r & 7) + 16 * (hh ^ ((r >> 2) & 3));
#define KRD(i) (kf[i] = *(const LAS bf16x8*)((lptr)(size_t)((((i) >> 1) & 1) ? kb : ka) + ((i) >> 2) * 512 + ((i) & 1) * 8192))
    bf16x8 kf[16];
    unsigned ka, kb, qad[8];
    {
        u64 eraw; unsigned cpf = 0;
        if (w == 0) asm volatile("global_load_dword %0, %1, off" : "=v"(cpf) : "v"(code0 + umin((idx * 10 + umin(lane, 9)) * 128, code_len)) : "memory");
        asm volatile("global_load_dwordx2 %0, %1, off sc1" : "=v"(eraw) : "v"(ws + WS_EPOCH + b * 256) : "memory");
        const unsigned woff = smb + LDS_W + (8 * rw + q4) * 256 + (((4 * w + (p >> 1)) ^ (2 * q4 + 8 * (rw & 1))) << 4) + 8 * (p & 1), wx = 32u;
        const unsigned xlo = smb + LDS_X + ch * 128 + ((rw ^ (ch >> 1)) << 4), xhi = xlo + 3 * 2048;
        f32x4 acc[12];
        GFrag fa, fb;
        unsigned wsl = 2, xsl = 0, wa0, wa1, xa0, xa1, xa2, xa3;
        GADDR();
        GSTEP(3, true, fa, fb, false, false);
        GSTEP(3, true, fb, fa, true, true);
#pragma unroll 1
        for (int i = 2; i < 16; i += 2) { GSTEP(3, true, fa, fb, true, false); GSTEP(3, true, fb, fa, true, false); }
        asm volatile("s_nop 7");
        const float cs = 0.08838834764831845f * 1.4426950408889634f;
        v2u qp[12];
        unsigned qa0, qa1;
        const unsigned qb0 = smb + LDS_X + w * 8192 + r * 256 + ((hh ^ (r & 15)) << 4);
#define QPK(n) do { qp[n] = v2u{pk2s(acc[n][0], acc[n][1], cs), pk2s(acc[n][2], acc[n][3], cs)}; PIN(qp[n]); } while (0)
#define QST(n) (*(LAS v2u*)((lptr)(size_t)(((n) & 1) ? qa1 : qa0) + ((n) >> 1) * 4096) = qp[n])
        SCHED0();
#pragma unroll
        for (int n = 0; n < 12; ++n) {
            GM(fb, 2 * n, false); SCHED0();
            if (n >= 3) QST(n - 3);
            if (n == 0) { qa0 = smb + LDS_X + ch * 256 + (((4 * w + (rw >> 1)) ^ ch) << 4) + 8 * (rw & 1); qa1 = qa0 ^ 32u; PIN(qa0); PIN(qa1); }
            if (n == 1) { ka = smb + 3 * WSLOT + kbase; kb = ka ^ 32u; PIN(ka); PIN(kb); }
            SCHED0();
            GM(fb, 2 * n + 1, false); SCHED0();
            if (n == 2) {
                WAIT_LGKM(0);
                BAR();
                if (cls) {
#pragma unroll
                    for (int i = 0; i < 16; ++i) KRD(i);
                }
                SCHED0();
            }
            if (n < 2) { _Pragma("unroll") for (int ks = 4 * n; ks < 4 * n + 4; ++ks) { qad[ks] = qb0 ^ (ks << 5); PIN(qad[ks]); } }
            if (n >= 2) QPK(n - 2);
            SCHED0();
        }
        QST(9); QPK(10); QPK(11); SCHED0(); QST(10); QST(11);
        asm volatile("s_waitcnt vmcnt(0)" : "+v"(eraw), "+v"(cpf) :: "memory");
        epoch = (((u64)__builtin_amdgcn_readfirstlane((unsigned)(eraw >> 32)) << 32) | (unsigned)__builtin_amdgcn_readfirstlane((unsigned)eraw)) + 0x9E3779B97F4A7C15ull;
        if (w == 3) { lds_st(sm, M_EPOCH, (unsigned)epoch); lds_st(sm, M_EPOCH + 1, (unsigned)(epoch >> 32)); }
    }
    if (w == 3) {
        WAIT_LGKM(0); BAR();
        const char* p_ = lane < 2 ? ws + WS_FLAG1 + (size_t)(b * 4 + hg) * 256 + lane * 128 : ws + WS_FLAG2 + (size_t)(b * 32 + hg * 8 + hl) * 256 + (lane & 1) * 128;
        const unsigned v_ = __hip_atomic_load((GAS unsigned*)p_, RLX_AGENT); asm volatile("" :: "v"(v_));
    }
    else {
        const unsigned vbase = 64 * (4 * hh + q4) + 16 * ((2 * blk + (p >> 1)) ^ hh) + 8 * (p & 1);
        bf16x8 qf[8]; s16x4 vlo[16], vhi[16]; v4u pw[4];
        f32x16 o[4], sa0, sa1, sb0, sb1, negm;
        float lsum = 0.f, ls2 = 0.f, xe = 0.f, xp = 0.f;
        unsigned fw = cls ? 4u : 1u, va, vb;
#define SLOT(t) (((unsigned)(SLOT_TAB >> (4u * (t))) & 15u) * WSLOT)
#define SET_K(t) do { ka = smb + SLOT(t) + kbase; kb = ka ^ 32u; PIN(ka); PIN(kb); } while (0)
#define SET_V(t) do { va = smb + SLOT(t) + vbase; vb = va ^ 32u; PIN(va); PIN(vb); } while (0)
#define VRD(i) do { vlo[i] = __builtin_amdgcn_ds_read_tr16_b64_v4i16((LAS s16x4*)((lptr)(size_t)va + ((i) & 3) * 512 + ((i) >> 2) * 4096));     \
                    vhi[i] = __builtin_amdgcn_ds_read_tr16_b64_v4i16((LAS s16x4*)((lptr)(size_t)vb + ((i) & 3) * 512 + ((i) >> 2) * 4096 + 2048)); } while (0)
#define PV(i) (o[(i) & 3] = MFMA32(__builtin_shufflevector(vlo[i], vhi[i], 0, 1, 2, 3, 4, 5, 6, 7), __builtin_bit_cast(bf16x8, pw[(i) >> 2]), o[(i) & 3]))
#define SV(N0, N1, e) (((e) < 16) ? N0[(e) & 15] : N1[(e) & 15])
#define USE(x) do { asm volatile("" :: "v"(x)); SCHED0(); } while (0)
#define FIN1(e) do { if ((e) & 1) { ls2 += xp; unsigned u_ = pk2(xe, xp); PIN(u_); pw[(e) >> 3][((e) >> 1) & 3] = u_; PIN(ls2); } else { lsum += xp; xe = xp; PIN(lsum); } } while (0)
#define EXP1(S0, S1, e) do { float x_ = __builtin_amdgcn_exp2f(SV(S0, S1, e)); PIN(x_); if ((e) > 0) FIN1((e) - 1); xp = x_; } while (0)
#define XHALF(mx) do { auto r2_ = __builtin_amdgcn_permlane32_swap(__float_as_uint(mx), __float_as_uint(mx), false, false); mx = max2(__uint_as_float(r2_[0]), __uint_as_float(r2_[1])); } while (0)
#define NEED(t) do { if (__builtin_expect(fw <= (t), 0)) { fw = lds_full(sm); lds_st(sm, M_PROG + w, (t) + 1u); wait_full(sm, (t) + 1u, fw); } } while (0)
        WAIT_LGKM(0);
        BAR();
        __builtin_amdgcn_s_setprio(3);
#pragma unroll
        for (int ks = 0; ks < 8; ++ks) qf[ks] = *(const LAS bf16x8*)(size_t)qad[ks];
        if (!cls) {
#pragma unroll
            for (int i = 0; i < 16; ++i) KRD((2 * i) % 16 + (i >> 3));
        }
        unsigned tk = 1u;
        float mx;
        SCHED0();
#pragma unroll
        for (int i = 0; i < 8; ++i) { sa0 = MFMA32(kf[2 * i], qf[i], i == 0 ? f32x16{} : sa0); SCHED0();
            _Pragma("unroll") for (int e = 0; e < 8; e += 2) { f32x2 z_ = {0.f, 0.f}; PIN(z_); o[i >> 1][8 * (i & 1) + e] = z_[0]; o[i >> 1][8 * (i & 1) + e + 1] = z_[1]; } SCHED0(); }
        lds_st(sm, M_PROG + w, cls ? 2u : 1u);
#pragma unroll
        for (int i = 0; i < 8; ++i) {
            sa1 = MFMA32(kf[2 * i + 1], qf[i], i == 0 ? f32x16{} : sa1); SCHED0();
            if (i >= 2 && i < 6) { mx = i == 2 ? max2(sa0[0], sa0[1]) : max3(mx, sa0[4 * i - 8], sa0[4 * i - 7]); mx = max3(mx, sa0[4 * i - 6], sa0[4 * i - 5]); PIN(mx); }
            SCHED0();
        }
        lds_st(sm, M_PROG + w, 2u);
        wait_full(sm, 2u, fw);
        SET_K(1u);
#pragma unroll
        for (int i = 0; i < 7; ++i) KRD(i);
        SCHED0();
        { float m2 = max3(sa1[0], sa1[1], sa1[2]);
#pragma unroll
          for (int e = 3; e < 9; e += 2) { m2 = max3(m2, sa1[e], sa1[e + 1]); mx = max3(mx, sa1[e + 6], sa1[e + 7]); }
          mx = max3(mx, m2, sa1[15]);
          XHALF(mx);
          sa0 -= mx; sa1 -= mx;
#pragma unroll
          for (int i = 0; i < 16; ++i) negm[i] = -mx; }
        PIN(negm);
        SCHED0();
#define STEP(S0, S1, N0, N1) do { v4u fv_; \
        _Pragma("unroll") for (int i = 0; i < 16; ++i) { \
            if (!(i & 1)) USE(kf[i + 1]); \
            if (i == 0) N0 = MFMA32(kf[0], qf[0], negm); else if (i == 1) N1 = MFMA32(kf[1], qf[0], negm); \
            else if (i & 1) N1 = MFMA32(kf[i], qf[i >> 1], N1); else N0 = MFMA32(kf[i], qf[i >> 1], N0); \
            SCHED0(); \
            if (i < 9) KRD(i + 7); \
            if (i == 4) fv_ = *(const volatile LAS v4u*)(sm + LDS_M + 4 * M_FULL); \
            if (i == 9) { const unsigned f_ = __builtin_amdgcn_readfirstlane(min4(fv_)); fw = fw > f_ ? fw : f_; NEED(tk + 1u); SET_V(tk + 1u); } \
            if (i >= 9) VRD(i - 9); \
            SCHED0(); \
            if (i < 8) { EXP1(S0, S1, i + (i >> 1)); if (i & 1) EXP1(S0, S1, i + (i >> 1) + 1); } else EXP1(S0, S1, i + 4); \
            SCHED0(); } \
        float mx_; \
        _Pragma("unroll") for (int i = 0; i < 16; ++i) { \
            if (!(i & 1)) USE(vhi[i + 1]); \
            PV(i); \
            SCHED0(); \
            if (i < 9) VRD(i + 7); \
            if (i == 9) { if (tk < 13u) NEED(tk + 2u); SET_K(tk + 2u); }     \
            if (i >= 9) KRD(i - 9); \
            SCHED0(); \
            if (i < 10) EXP1(S0, S1, 20 + i); \
            if (i == 10) { EXP1(S0, S1, 30); EXP1(S0, S1, 31); FIN1(31); } \
            if (i == 11) { mx_ = max2(N0[0], N0[1]); mx_ = max3(mx_, N0[2], N0[3]); mx_ = max3(mx_, N0[4], N0[5]); mx_ = max3(mx_, N0[6], N0[7]); PIN(mx_); } \
            if (i >= 12 && i < 15) { _Pragma("unroll") for (int e = 8 * (i - 11); e < 8 * (i - 10); e += 2) mx_ = max3(mx_, SV(N0, N1, e), SV(N0, N1, e + 1)); PIN(mx_); } \
            if (i == 13) { S0 = N0; PIN(S0); } \
            if (i == 15) { S1 = N1; PIN(S1); } \
            SCHED0(); } \
        tk += 2u; \
        lds_st(sm, M_PROG + w, tk + 1u); \
        if (__builtin_expect(__builtin_amdgcn_ballot_w64(mx_ > THR) != 0ull, 0)) {     \
            XHALF(mx_); \
            const float dl_ = max2(mx_, 0.f), al_ = __builtin_amdgcn_exp2f(-dl_); \
            lsum *= al_; ls2 *= al_; \
            _Pragma("unroll") for (int i = 0; i < 16; ++i) { S0[i] -= dl_; S1[i] -= dl_; negm[i] -= dl_; o[0][i] *= al_; o[1][i] *= al_; o[2][i] *= al_; o[3][i] *= al_; } } \
        SCHED0(); } while (0)
#pragma unroll 1
        while (tk < 15u) STEP(sa0, sa1, sb0, sb1);
        const unsigned pgv = *(const volatile LAS unsigned*)(sm + LDS_M + 4 * (M_PROG + (lane & 3)));
#pragma unroll
        for (int e = 0; e < 6; ++e) EXP1(sa0, sa1, e);
        wait_full(sm, 16u, fw);
        SET_V(15u);
        SCHED0();
#define TI(i) ((i) < 8 ? (i) : 8 + 4 * ((i) & 1) + (((i) - 8) >> 1))
#pragma unroll
        for (int i = 0; i < 7; ++i) VRD(TI(i));
#pragma unroll
        for (int e = 6; e < 16; ++e) EXP1(sa0, sa1, e);
        if (__builtin_amdgcn_ballot_w64(pgv < 10u) != 0ull) wait_prog(sm, 10u, lane);
        const int row = 32 * w + r;
        const unsigned oa = smb + LDS_X + row * 128 + 8 * hh + (((row >> 1) & 7) << 4);
        unsigned oad[8];
        v2u op[16];
        float inv;
#define OPK(i) do { op[i] = v2u{pk2s(o[(i) >> 2][4 * ((i) & 3)], o[(i) >> 2][4 * ((i) & 3) + 1], inv), pk2s(o[(i) >> 2][4 * ((i) & 3) + 2], o[(i) >> 2][4 * ((i) & 3) + 3], inv)}; PIN(op[i]); } while (0)
#define OST(i) (*(LAS v2u*)((lptr)(size_t)oad[(i) & 7] + ((i) >> 3) * XSLOT) = op[i])
        SCHED0();
#pragma unroll
        for (int i = 0; i < 16; ++i) {
            if (!(i & 1)) USE(vhi[TI(i + 1)]);
            PV(TI(i));
            SCHED0();
            if (i < 9) VRD(TI(i + 7));
            if (i >= 12) OST(i - 12);
            SCHED0();
            if (i < 8) { EXP1(sa0, sa1, 16 + 2 * i); EXP1(sa0, sa1, 17 + 2 * i); }
            if (i == 7) FIN1(31);
            if (i == 8 || i == 10) { _Pragma("unroll") for (int g = 2 * i - 16; g < 2 * i - 12; ++g) { oad[g] = oa ^ (g << 4); PIN(oad[g]); } }
            if (i == 9) { lsum += ls2; auto r2 = __builtin_amdgcn_permlane32_swap(__float_as_uint(lsum), __float_as_uint(lsum), false, false); inv = __builtin_amdgcn_rcpf(__uint_as_float(r2[0]) + __uint_as_float(r2[1])); PIN(inv); }
            if (i >= 11) OPK(i - 11);
            SCHED0();
        }
        lds_st(sm, M_PROG + w, 17u);
        OPK(5); OPK(6); OPK(7); OPK(8); SCHED0(); OST(4); OST(5); OST(6); OST(7); SCHED0();
        OPK(9); OPK(10); OPK(11); OPK(12); SCHED0(); OST(8); OST(9); OST(10); OST(11); SCHED0();
        OPK(13); OPK(14); OPK(15); SCHED0(); OST(12); OST(13); OST(14); OST(15);
    }
    {
        const unsigned woff = smb + LDS_W + (8 * rw + q4) * 256 + (((8 * (hg >> 1) + 2 * w + (p >> 1)) ^ (2 * q4 + 8 * (rw & 1))) << 4) + 8 * (p & 1), wx = 128u;
        const unsigned xb = smb + LDS_X + ch * 128 + ((rw ^ (ch >> 1)) << 4), xlo = xb + (hg & 1) * 3 * 2048, xhi = xb + ((hg & 1) ^ 1) * 3 * 2048;
        f32x4 acc[12];
        GFrag fa, fb;
        unsigned wsl = 3, xsl = 0, wa0, wa1, xa0, xa1, xa2, xa3;
        GADDR();
        __builtin_amdgcn_s_setprio(0);
        WAIT_LGKM(0);
        BAR();
        GSTEP(7, false, fa, fb, false, false);
        GSTEP(7, false, fb, fa, true, true);
#pragma unroll 1
        for (int i = 2; i < 16; i += 2) { GSTEP(7, true, fa, fb, true, false); GSTEP(7, true, fb, fa, true, false); }
        asm volatile("s_nop 7");
        const rsrc_t rP = __builtin_amdgcn_make_buffer_rsrc((void*)(ws + WS_PART + ((size_t)(b * 32 + hl) * 4 + w) * 12288), 0, 25 * 4 * 12288, 0x00020000);
        GAS u64* f2 = (GAS u64*)(ws + WS_FLAG2 + ((size_t)(b * 32 + hl) * 4 + w) * 64);
#define AT(j, k) acc[2 * (3 * ((j) & 1) + (k)) + ((j) >> 1)]
#define PST(j) do { const unsigned so_ = (hg ^ (j)) * (8 * 4 * 12288) + hg * 3072; \
            __builtin_amdgcn_raw_buffer_store_b128(v4u{pk2(AT(j, 0)[0], AT(j, 0)[1]), pk2(AT(j, 0)[2], AT(j, 0)[3]), pk2(AT(j, 1)[0], AT(j, 1)[1]), pk2(AT(j, 1)[2], AT(j, 1)[3])}, rP, lane * 16, so_, 16); \
            __builtin_amdgcn_raw_buffer_store_b64(v2u{pk2(AT(j, 2)[0], AT(j, 2)[1]), pk2(AT(j, 2)[2], AT(j, 2)[3])}, rP, lane * 8 + 1024, so_, 16); } while (0)
        SCHED0();
#pragma unroll
        for (int e = 0; e < 12; ++e) {
            const int j = e < 3 ? 2 : e < 6 ? 1 : e < 9 ? 3 : 0, n = 2 * (3 * (j & 1) + e % 3) + (j >> 1);
            GM(fb, 2 * n, false); GM(fb, 2 * n + 1, false); SCHED0();
            if (e == 4) PST(2);
            if (e == 7) PST(1);
            if (e == 10) PST(3);
            SCHED0();
        }
        WAIT_VM(0);
        if (lane < 4 && lane != hg) put_flag(f2 + lane * (8 * 4 * 8) + hg, epoch);
        wait_flags(f2 + hg * (8 * 4 * 8) + ((lane & 3) == hg ? ((hg + 1) & 3) : (lane & 3)), epoch);
        v4u pt[3]; v2u pc[3];
#pragma unroll
        for (int i = 0; i < 3; ++i) {
            const unsigned so = hg * (8 * 4 * 12288) + (hg ^ (i + 1)) * 3072;
            pt[i] = __builtin_amdgcn_raw_buffer_load_b128(rP, lane * 16, so, 16);
            pc[i] = __builtin_amdgcn_raw_buffer_load_b64(rP, lane * 8 + 1024, so, 16);
        }
        char* orow = outg + (size_t)(b * 96 + 48 * (hg & 1) + ch) * 2048 + (nt * 128 + 64 * (hg >> 1) + 16 * w + 4 * rw) * 2;
#pragma unroll
        for (int k = 0; k < 3; ++k) {
            f32x4 v = AT(0, k);
#pragma unroll
            for (int i = 0; i < 3; ++i) { const unsigned u0 = k < 2 ? pt[i][2 * k] : pc[i][0], u1 = k < 2 ? pt[i][2 * k + 1] : pc[i][1];
                v += f32x4{__uint_as_float(u0 << 16), __uint_as_float(u0 & 0xffff0000u), __uint_as_float(u1 << 16), __uint_as_float(u1 & 0xffff0000u)}; }
            *(GAS v2u*)(orow + (size_t)k * 16 * 2048) = v2u{pk2(v[0], v[1]), pk2(v[2], v[3])};
        }
        asm volatile(".Lmha_code_end:");
        if (hg == 0 && hl == 0 && w == 0 && lane == 0) __hip_atomic_store((GAS u64*)(ws + WS_EPOCH + b * 256), epoch, RLX_AGENT);
    }
}

extern "C" void kernel_launch(void* const* d_in, const int* in_sizes, int n_in, void* d_out, int out_size, void* d_ws, size_t ws_size, hipStream_t stream) {
    mha_fused<<<256, 512, 0, stream>>>((const char*)d_in[0], (const char*)d_in[1], (const char*)d_in[2], (const char*)d_in[3], (const char*)d_in[4], (char*)d_out, (char*)d_ws);
}
```
